```python
import torch

def _softmax(x, axis=-1):
    return torch.softmax(x, dim=axis)

def _gelu(x):
    return torch.nn.functional.gelu(x, approximate='tanh')

def _silu(x):
    return torch.nn.functional.silu(x)

def _sigmoid(x):
    return torch.sigmoid(x)

def _layernorm(x, axis=-1, eps=1e-05):
    m = x.mean(dim=axis, keepdim=True)
    v = x.var(dim=axis, keepdim=True, unbiased=False)
    return (x - m) / torch.sqrt(v + eps)

def _scatter_add(tgt, idx, src, axis=0):
    return tgt.clone().index_add_(axis, idx, src)

def _segment_sum(src, seg, n):
    out = torch.zeros((n,) + src.shape[1:], dtype=src.dtype)
    return out.index_add_(0, seg, src)

def _pad(x, pw):
    flat = tuple((v for pair in reversed(pw) for v in pair))
    return torch.nn.functional.pad(x, flat)
kernel_name = 'adaLN_cond__memory_aug_recurrent_mem_tr__swiglu_ffn__prompt'

def setup_inputs():

    _w = lambda a: (lambda t: t.to(torch.bfloat16) if t.is_floating_point() else t)(torch.as_tensor(a))

    def _A_setup():
        B, S, Dm = (1, 2048, 640)
        H, Dh = (32, 128)
        M, seg = (8, 32)
        seg = min(seg, S)
        S = S // seg * seg
        return {'x': _w((lambda *s: torch.randn(*s, dtype=torch.bfloat16) * 0.05)(B, S, Dm)), 'mem0': _w((lambda *s: torch.randn(*s, dtype=torch.bfloat16) * 0.4)(B, M, Dm)), 'Wq': _w((lambda *s: torch.randn(*s, dtype=torch.bfloat16) * 3.2)(Dm, H, Dh) * Dm ** (-0.5)), 'Wk': _w((lambda *s: torch.randn(*s, dtype=torch.bfloat16) * 3.2)(Dm, H, Dh) * Dm ** (-0.5)), 'Wv': _w((lambda *s: torch.randn(*s, dtype=torch.bfloat16) * 0.4)(Dm, H, Dh) * Dm ** (-0.5)), 'Wo': _w((lambda *s: torch.randn(*s, dtype=torch.bfloat16) * 0.4)(H, Dh, Dm) * (H * Dh) ** (-0.5)), 'seg_len': seg}
    _A_in = _A_setup()

    def _B_setup():
        B, S, D, F = (1, 2048, 640, 1728)
        return {'x': _w((lambda *s: torch.randn(*s, dtype=torch.bfloat16) * 0.05)(B, S, D)), 'w_gate': _w((lambda *s: torch.randn(*s, dtype=torch.bfloat16) * 0.05)(D, F)), 'w_up': _w((lambda *s: torch.randn(*s, dtype=torch.bfloat16) * 0.05)(D, F)), 'w_down': _w((lambda *s: torch.randn(*s, dtype=torch.bfloat16) * 0.05)(F, D))}
    _B_in = _B_setup()
    return {'A_x': _A_in['x'], 'A_mem0': _A_in['mem0'], 'A_Wq': _A_in['Wq'], 'A_Wk': _A_in['Wk'], 'A_Wv': _A_in['Wv'], 'A_Wo': _A_in['Wo'], 'A_seg_len': _A_in['seg_len'], 'B_x': _B_in['x'], 'B_w_gate': _B_in['w_gate'], 'B_w_up': _B_in['w_up'], 'B_w_down': _B_in['w_down'], 'c': _w((lambda *s: torch.randn(*s, dtype=torch.bfloat16) * 0.1)(1, 640) * 8.0), 'w_mod': _w((lambda *s: torch.randn(*s, dtype=torch.bfloat16) * 0.1)(640, 6 * 640) * 640 ** (-0.5) * 8.0)}

def reference(A_x, A_mem0, A_Wq, A_Wk, A_Wv, A_Wo, A_seg_len, B_x, B_w_gate, B_w_up, B_w_down, c, w_mod):

    def _A_ref(x, mem0, Wq, Wk, Wv, Wo, seg_len):
        B, S, Dm = x.shape
        H, Dh = (Wq.shape[1], Wq.shape[2])
        M = mem0.shape[1]
        scale = Dh ** (-0.5)
        n_seg = S // seg_len
        L = M + seg_len
        idx = torch.arange(L)
        mask = (idx[:, None] < M) | (idx[:, None] >= idx[None, :])
        mem = mem0
        outs = []
        for s in range(n_seg):
            xs = x[:, s * seg_len:(s + 1) * seg_len, :].clone()
            h = torch.concatenate([mem, xs], axis=1)
            q = torch.einsum('btd,dhe->bhte', h, Wq)
            k = torch.einsum('btd,dhe->bhte', h, Wk)
            v = torch.einsum('btd,dhe->bhte', h, Wv)
            scores = torch.einsum('bhqe,bhke->bhqk', q, k) * scale
            scores = torch.where(mask[None, None], scores, -1000000000.0)
            p = _softmax(scores, axis=-1)
            o = torch.einsum('bhqk,bhke->bhqe', p, v)
            o = torch.einsum('bhte,hed->btd', o, Wo)
            mem = o[:, :M, :].clone()
            outs.append(o[:, M:, :])
        return torch.concatenate(outs, axis=1)

    def _B_ref(x, w_gate, w_up, w_down):
        g = _silu(x @ w_gate)
        u = x @ w_up
        return g * u @ w_down
    x = A_x
    mod = (_silu(c) @ w_mod).reshape(c.shape[0], 6, 1, -1)
    sa, ca, ga, sb, cb, gb = (mod[:, 0].clone(), mod[:, 1].clone(), mod[:, 2].clone(), mod[:, 3].clone(), mod[:, 4].clone(), mod[:, 5].clone())
    h = _layernorm(x, axis=-1) * (1 + ca) + sa
    x = x + ga * _A_ref(h, A_mem0, A_Wq, A_Wk, A_Wv, A_Wo, A_seg_len)
    h = _layernorm(x, axis=-1) * (1 + cb) + sb
    x = x + gb * _B_ref(h, B_w_gate, B_w_up, B_w_down)
    return x
```

```cpp
#include <hip/hip_runtime.h>
#include <stdint.h>
#include <type_traits>
#define GAS __attribute__((address_space(1)))
#define LAS __attribute__((address_space(3)))
typedef __bf16 bf16x2 __attribute__((ext_vector_type(2)));
typedef __bf16 bf16x8 __attribute__((ext_vector_type(8)));
typedef short s16x4 __attribute__((ext_vector_type(4)));
typedef float f32x2 __attribute__((ext_vector_type(2)));
typedef float f32x4 __attribute__((ext_vector_type(4)));
typedef unsigned u32x2 __attribute__((ext_vector_type(2)));
typedef unsigned u32x4 __attribute__((ext_vector_type(4)));
typedef unsigned u32x8 __attribute__((ext_vector_type(8)));
typedef unsigned u32x16 __attribute__((ext_vector_type(16)));
typedef unsigned u32x32 __attribute__((ext_vector_type(32)));
typedef GAS unsigned gu32;
typedef __amdgpu_buffer_rsrc_t rsrc_t;
typedef unsigned char uchar;
#define RLX_AGENT __ATOMIC_RELAXED, __HIP_MEMORY_SCOPE_AGENT
#define DEVI __device__ __forceinline__

constexpr unsigned PAGE = 2u << 20;
constexpr unsigned SENT = 0xFFFFFFFFu, SENTA = 0xff81ff81u, OOB = 0x7fffffffu, BIG = 0x7fff0000u;
constexpr unsigned LDS_BYTES = 163840;
constexpr unsigned SPIN = 1u << 18;
constexpr unsigned F_H = 8192, W_MOD = 16384, F_KV = 32768, F_OP = 36864, F_M = 49152, F_H2 = 65536, F_FP = 81920, F_BYTES = 98304;
constexpr unsigned W_H = 131072, ROWS_B = 2048 * 1280;
constexpr unsigned W_ST = W_H + ROWS_B, W_END = 2 * PAGE, G_DONE = 4096, G_ACK = F_BYTES, G_REQ = W_ST + 0x60000, B_Z = 15 * 8 + 4;
constexpr unsigned P_SEG = 4 * PAGE, P_END = 100 * PAGE, O_Q = 0x100000, O_KVM = 0x180000, UN = 81920;
constexpr unsigned NINIT = 4, T_TG = 0;
constexpr unsigned T_XL = 10, T_RX = 10, T_TOL = 16, T_REC = 20, T_UP = 2, T_DN = 2, T_RC = 36, T_RD = 20;
__host__ __device__ constexpr unsigned PH(unsigned o) { return ((o >> 13) << 14) | (o & 8191); }

struct Prm {
    const uchar *x, *mem0, *Wq, *Wk, *Wv, *Wo, *Wg, *Wu, *Wd, *c, *wmod;
    uchar *out, *ws, *comm;
    int tA, tB, dG, flip;
};

DEVI unsigned now32() { unsigned long long t; asm volatile("s_memtime %0\n\ts_waitcnt lgkmcnt(0)" : "=s"(t) :: "memory"); return (unsigned)t; }
DEVI void glds16(const void* gsrc, unsigned lds_dst) {
    unsigned keep;
    asm volatile("s_mov_b32 %0, m0\n\ts_mov_b32 m0, %2\n\ts_nop 0\n\tglobal_load_lds_dwordx4 %1, off\n\ts_mov_b32 m0, %0" : "=&s"(keep) : "v"(gsrc), "s"(lds_dst) : "memory");
}
#define LDS_BAR() do { asm volatile("s_waitcnt lgkmcnt(0)" ::: "memory"); __builtin_amdgcn_s_barrier(); asm volatile("" ::: "memory"); } while (0)
#define VM_BAR(n) do { asm volatile("s_waitcnt vmcnt(" #n ") lgkmcnt(0)" ::: "memory"); __builtin_amdgcn_s_barrier(); asm volatile("" ::: "memory"); } while (0)
#define BAR() do { asm volatile("" ::: "memory"); __builtin_amdgcn_s_barrier(); asm volatile("" ::: "memory"); } while (0)
#define VM_WAIT0() asm volatile("s_waitcnt vmcnt(0)" ::: "memory")
#define LP(T, a, o) ((LAS T*)((LAS uchar*)(uintptr_t)(a) + (o)))
DEVI u32x2 trd(unsigned a, unsigned o) { return __builtin_bit_cast(u32x2, __builtin_amdgcn_ds_read_tr16_b64_v4i16(LP(s16x4, a, o))); }
DEVI u32x4 trd2(unsigned a, unsigned oa, unsigned b, unsigned ob) { u32x2 l = trd(a, oa), h = trd(b, ob); return u32x4{l[0], l[1], h[0], h[1]}; }
DEVI u32x4 ldsr128(unsigned a, unsigned o) { return *LP(u32x4, a, o); }
DEVI f32x2 ldsrf2(unsigned a, unsigned o) { return *LP(f32x2, a, o); }
DEVI float ldsrf(unsigned a, unsigned o) { return *LP(float, a, o); }
DEVI void ldsw128(unsigned a, unsigned o, u32x4 v) { *LP(u32x4, a, o) = v; }
DEVI void ldsw64(unsigned a, unsigned o, u32x2 v) { *LP(u32x2, a, o) = v; }
DEVI void ldswf(unsigned a, unsigned o, float v) { *LP(float, a, o) = v; }
DEVI unsigned opq(unsigned x) { asm volatile("" : "+v"(x)); return x; }
DEVI unsigned pk2(float a, float b) { f32x2 f = {a, b}; return __builtin_bit_cast(unsigned, __builtin_convertvector(f, bf16x2)); }
DEVI u32x4 pk8(f32x4 a, f32x4 b) { return u32x4{pk2(a[0], a[1]), pk2(a[2], a[3]), pk2(b[0], b[1]), pk2(b[2], b[3])}; }
DEVI float blo(unsigned u) { return __uint_as_float(u << 16); }
DEVI float bhi(unsigned u) { return __uint_as_float(u & 0xffff0000u); }
DEVI float rbf(float a) { return blo(pk2(a, a)); }
DEVI f32x4 mf32(u32x4 a, u32x4 b, f32x4 c) { return __builtin_amdgcn_mfma_f32_16x16x32_bf16(__builtin_bit_cast(bf16x8, a), __builtin_bit_cast(bf16x8, b), c, 0, 0, 0); }
DEVI f32x4 mf16(u32x2 a, u32x2 b, f32x4 c) { return __builtin_amdgcn_mfma_f32_16x16x16bf16_1k(__builtin_bit_cast(s16x4, a), __builtin_bit_cast(s16x4, b), c, 0, 0, 0); }
DEVI float xmax4(float x) {
    unsigned u = __float_as_uint(x);
    auto r = __builtin_amdgcn_permlane32_swap(u, u, false, false);
    u = __float_as_uint(fmaxf(__uint_as_float(r[0]), __uint_as_float(r[1])));
    auto q = __builtin_amdgcn_permlane16_swap(u, u, false, false);
    return fmaxf(__uint_as_float(q[0]), __uint_as_float(q[1]));
}
DEVI float xsum4(float x) {
    unsigned u = __float_as_uint(x);
    auto r = __builtin_amdgcn_permlane32_swap(u, u, false, false);
    u = __float_as_uint(__uint_as_float(r[0]) + __uint_as_float(r[1]));
    auto q = __builtin_amdgcn_permlane16_swap(u, u, false, false);
    return __uint_as_float(q[0]) + __uint_as_float(q[1]);
}
template <int C> DEVI float dpp(float v) { return __uint_as_float(__builtin_amdgcn_update_dpp(0, __float_as_uint(v), C, 0xf, 0xf, true)); }
DEVI float rsum16(float x) {
    x += dpp<0xb1>(x); x += dpp<0x4e>(x); x += dpp<0x141>(x); x += dpp<0x140>(x); return x;
}
DEVI unsigned ld32(rsrc_t r, unsigned v, unsigned s) { return __builtin_amdgcn_raw_buffer_load_b32(r, v, s, 16); }
DEVI u32x2 ld64(rsrc_t r, unsigned v, unsigned s) { return __builtin_amdgcn_raw_buffer_load_b64(r, v, s, 16); }
DEVI u32x4 ld128(rsrc_t r, unsigned v, unsigned s) { return __builtin_amdgcn_raw_buffer_load_b128(r, v, s, 16); }
DEVI void st32(rsrc_t r, unsigned v, unsigned s, unsigned d) { __builtin_amdgcn_raw_buffer_store_b32(d, r, v, s, 16); }
DEVI void st64(rsrc_t r, unsigned v, unsigned s, u32x2 d) { __builtin_amdgcn_raw_buffer_store_b64(d, r, v, s, 16); }
DEVI void st128m(rsrc_t r, unsigned v, unsigned s, u32x4 d) { __builtin_amdgcn_raw_buffer_store_b128(d, r, v, s, 16); }
DEVI void st128(rsrc_t r, unsigned v, unsigned s, u32x4 d) { st128m(r, v, s, d); asm volatile("s_nop 1" :: "v"(d)); }
DEVI unsigned wait_ge(rsrc_t r, unsigned& budget, unsigned voff, unsigned val, int slow = 0) {
    unsigned f;
#pragma unroll 1
    for (;;) {
        asm volatile("" ::: "memory");
        f = ld32(r, voff, 0);
        if (__builtin_amdgcn_ballot_w64(f < val) == 0 || budget == 0) break;
        --budget;
        __builtin_amdgcn_s_sleep(2);
        if (slow) __builtin_amdgcn_s_sleep(20);
    }
    asm volatile("" ::: "memory");
    return f;
}

struct Cd { unsigned c, a; };
DEVI unsigned gw(unsigned k) { return blockIdx.x * k + (threadIdx.x >> 6) * 4; }
DEVI void ask(rsrc_t r, const Cd& cd) { st32(r, (threadIdx.x & 63) == 0 ? gw(128) : OOB, G_REQ, cd.a + 1); asm volatile("" ::: "memory"); }
DEVI void gate(rsrc_t r, const Cd& cd) {
#pragma unroll 1
    for (unsigned bud = SPIN; bud != 0; --bud) {
        asm volatile("" ::: "memory");
        if (__builtin_amdgcn_ballot_w64(ld32(r, gw(128), G_ACK) != cd.a + 1) == 0) break;
        __builtin_amdgcn_s_sleep(2);
    }
    asm volatile("" ::: "memory");
    st32(r, (threadIdx.x & 63) == 0 ? gw(16) : OOB, G_DONE, 1);
}
DEVI void echo(rsrc_t r, unsigned n, u32x4 a) {
#pragma unroll 1
    for (; n != 0; --n) {
        asm volatile("" ::: "memory");
        const u32x4 q = ld128(r, threadIdx.x * 128, G_REQ), d = ld128(r, n > 64 ? threadIdx.x * 16 : OOB, G_DONE), e = q ^ a;
        st128(r, (e[0] | e[1] | e[2] | e[3]) ? threadIdx.x * 128 : OOB, G_ACK, q);
        a = q;
        if (__builtin_amdgcn_ballot_w64((d[0] & d[1] & d[2] & d[3]) == 0 && threadIdx.x != B_Z) == 0) break;
    }
}
DEVI unsigned touch(const uchar* a) { return *(const GAS unsigned*)a; }
DEVI unsigned touch_w(const uchar* w, int x, int n) { return touch(w + (size_t)(n >> 4) * 8192 + x * 2048 + (n & 15) * 128); }
DEVI const uchar* kvw(const Prm& p, int ty) {
    const uchar *k = p.Wk, *v = p.Wv, *q = p.Wq;
    asm("" : "+s"(k), "+s"(v), "+s"(q));
    return ty == 0 ? k : ty == 1 ? v : q;
}
constexpr int C_RND = 2;
DEVI unsigned touch_c(const Prm& p, int x, int c) {
    if (c >= 120) return touch(p.Wo + (size_t)x * 8 * 163840 + ((c - 120) * 256 + threadIdx.x) * 128);
    return touch_w(kvw(p, c < 80 ? c & 1 : 2), x, (c < 40 ? 20 + (c >> 1) : c < 80 ? (c >> 1) - 20 : c - 80) * 256 + threadIdx.x);
}
template <int N> DEVI void touched(const unsigned (&v)[N], unsigned u = 0) {
    asm volatile("" :: "v"(u));
#pragma unroll
    for (int i = 0; i < N; ++i) asm volatile("" :: "v"(v[i]));
}

constexpr unsigned L_QI = 0, L_VI = 8192, L_WO = 81920;
#define LO(p) ((unsigned)(uintptr_t)(p))
#define HI(p) ((unsigned)((uintptr_t)(p) >> 32))
#define CL10(p, a) p #a "0", p #a "1", p #a "2", p #a "3", p #a "4", p #a "5", p #a "6", p #a "7", p #a "8", p #a "9"
#define CL100(p, a) CL10(p, a##0), CL10(p, a##1), CL10(p, a##2), CL10(p, a##3), CL10(p, a##4), CL10(p, a##5), CL10(p, a##6), CL10(p, a##7), CL10(p, a##8), CL10(p, a##9)
#define CL_60_255(p) CL10(p, 6), CL10(p, 7), CL10(p, 8), CL10(p, 9), CL100(p, 1), CL10(p, 20), CL10(p, 21), CL10(p, 22), CL10(p, 23), CL10(p, 24), p "250", p "251", p "252", p "253", p "254", p "255"

DEVI void chain_role(const Prm& p, rsrc_t r, const int role, const Cd& cd) {
    const int tid = threadIdx.x, lane = tid & 63, w = __builtin_amdgcn_readfirstlane(tid >> 6);
    const int h = role >> 1, c = role & 1;
    const int g = lane >> 4, c16 = lane & 15, q4 = (lane >> 2) & 3, p4 = lane & 3;
    unsigned tc[C_RND];
#pragma unroll
    for (int i = 0; i < C_RND; ++i) tc[i] = touch_c(p, role >> 4, 31 * i + (role & 15));
    ask(r, cd);
    u32x16 cv; u32x32 pv;
    cv[0] = lane * 16;
    cv[1] = (lane & 2) ? 0xEEEE : 0x4444;
    cv[2] = L_QI + (16 * w + c16) * 32 + (2 * w + (c16 >> 3)) * 128 + g * 8;
    cv[3] = L_QI + (8 * g + q4) * 32 + g * 128 + p4 * 8;
    cv[4] = w * 1024 + lane * 16;
    cv[5] = cd.a + 1;
    cv[6] = L_VI + (16 * w + c16) * 16 + g * 4;
    cv[7] = L_VI + c16 * 16 + g * 4;
    cv[8] = L_WO + w * 20480 + lane * 16;
#pragma unroll
    for (int I = 0; I < 3; ++I) {
        const int j = I < 2 ? 16 * I + 4 * w + (c16 >> 2) : 32 + 2 * w + (c16 >> 3), un = 2 * j + (g >> 1);
        cv[9 + I] = un * 16384 + ((role + 5 * un) & 63) * 128 + ((g & 1) * 4 + (c16 & 3)) * 16 + (I == 2 ? ((c16 >> 2) & 1) * 8 : 0);
    }
    cv[12] = tid == 0 ? F_M + role * 128 : OOB;
    cv[13] = w * 512 + lane * 8;
    cv[14] = 0xbe0293eeu;
    cv[15] = lane * 16;
#pragma unroll
    for (int i = 0; i < 4; ++i) {
        const int row = 16 * w + g + 4 * i, f = (row & 1) | (((row >> 2) & 3) << 1);
        pv[i] = row * 8192 + ((c16 ^ (f << 1)) * 16);
    }
#pragma unroll
    for (int i = 0; i < 2; ++i) {
        const int row = 16 * w + 8 * i + (lane >> 3), f = (row >> 2) & 3;
        pv[4 + i] = row * 8192 + (((lane & 7) ^ (f << 1)) * 16);
        const int rl = 8 * w + 4 * i + g, fo = (rl & 1) | (((rl >> 4) & 1) << 3);
        pv[6 + i] = rl * 1280 + ((c16 ^ fo) * 16);
    }
    {
        const int rrow = 16 * (g >> 1) + 8 * (q4 >> 1) + 4 * (g & 1) + (q4 & 1);
        const int fq = (rrow & 1) | (((rrow >> 2) & 3) << 1);
        pv[8] = rrow * 256 + (((2 * w + (p4 >> 1)) ^ (fq << 1)) * 16) + (p4 & 1) * 8;
        pv[9] = rrow * 256 + (((8 + 2 * w + (p4 >> 1)) ^ (fq << 1)) * 16) + (p4 & 1) * 8;
        pv[10] = rrow * 128 + (((2 * w + (p4 >> 1)) ^ (((rrow >> 2) & 3) << 1)) * 16) + (p4 & 1) * 8;
    }
    pv[11] = 0;
    {
        const int rl = 16 * (g & 1) + 4 * (g >> 1) + (q4 & 1);
        const int f = (q4 & 1) | ((g & 1) << 3);
#pragma unroll
        for (int ta = 0; ta < 10; ++ta) {
            const int tau = (ta & ~1) + (q4 >> 1);
            const unsigned rbase = (((q4 >> 1) ^ ta) & 1) * 40960 + rl * 256;
            int d0;
            if (tau < 8) d0 = 16 * (16 * (tau >> 2) + 4 * w + p4) + 4 * (tau & 3);
            else d0 = 16 * (32 + 2 * w + (p4 >> 1)) + 4 * (2 * (p4 & 1) + (tau - 8));
            const unsigned byte = d0 * 2, cb = byte >> 8, ch = (byte >> 4) & 15;
            pv[12 + ta] = rbase + cb * 8192 + ((ch ^ f) * 16) + (byte & 8);
        }
    }
#pragma unroll
    for (int i = 22; i < 32; ++i) pv[i] = 0;
    u32x16 sa, sb; u32x8 sc;
    { const uchar* q = p.Wq + h * 256; sa[0] = LO(q); sa[1] = HI(q); }
    { const uchar* q = p.Wk + h * 256; sa[2] = LO(q); sa[3] = HI(q); }
    { const uchar* q = p.Wv + h * 256 + c * 128; sa[4] = LO(q); sa[5] = HI(q); }
    { const uchar* q = p.Wo + (h * 128 + c * 64) * 1280; sa[6] = LO(q); sa[7] = HI(q); }
    { const uchar* q = p.ws + W_ST + 65536 + (role * 4 + w) * 1024; sa[8] = LO(q); sa[9] = HI(q); }
    sa[10] = (G_ACK + gw(128)) | (G_DONE + gw(16)) << 17; sa[11] = p.dG & 0xffff; sa[12] = w; sa[13] = role; sa[14] = (p.tB >> 8) & 511; sa[15] = p.tB & 255;
    sb[0] = LO(p.comm); sb[1] = HI(p.comm); sb[2] = 4 * PAGE; sb[3] = 0x00020000;
    { const uchar* q = p.comm + P_SEG + h * 32768; sb[4] = LO(q); sb[5] = HI(q); sb[6] = 96 * PAGE; sb[7] = 0x00020000; }
    { const uchar* q = p.comm + P_SEG + O_KVM - 32768; sb[8] = LO(q); sb[9] = HI(q); sb[10] = 96 * PAGE; sb[11] = 0x00020000; }
    sb[12] = LO(p.ws); sb[13] = HI(p.ws); sb[14] = W_H; sb[15] = 0x00020000;
    sc[0] = (role >> 4) * 256 + ((role & 15) ? 0 : 64 << 16); sc[1] = F_KV + (h >> 1) * 128; sc[2] = w == 0; sc[3] = w == 0 ? 31 : 1; sc[4] = (p.tB >> 17) & 31;
    sc[5] = (p.tB >> 22) & 15; sc[6] = h * 16384 + c * 4096 + 32768 + 8192; sc[7] = (NINIT << 8) | (1 ^ ((p.flip >> 4) & 1));
#pragma unroll
    for (int i = 0; i < 16; ++i) { sa[i] = __builtin_amdgcn_readfirstlane(sa[i]); sb[i] = __builtin_amdgcn_readfirstlane(sb[i]); }
#pragma unroll
    for (int i = 0; i < 8; ++i) sc[i] = __builtin_amdgcn_readfirstlane(sc[i]);
    u32x16 pv0 = pv.lo, pv1 = pv.hi;
    touched(tc, cd.c);
    asm volatile(
    "s_lshl_b32 s82, s48, 12\n"
    "s_lshl_b32 s83, s48, 11\n"
    "s_add_u32 m0, s82, 0\n"
    "s_nop 0\n"
    "global_load_lds_dwordx4 v20, s[36:37]\n"
    "s_add_u32 m0, s82, 1024\n"
    "s_nop 0\n"
    "global_load_lds_dwordx4 v21, s[36:37]\n"
    "s_add_u32 m0, s82, 2048\n"
    "s_nop 0\n"
    "global_load_lds_dwordx4 v22, s[36:37]\n"
    "s_add_u32 m0, s82, 3072\n"
    "s_nop 0\n"
    "global_load_lds_dwordx4 v23, s[36:37]\n"
    "s_add_u32 s36, s36, 524288\n"
    "s_addc_u32 s37, s37, 0\n"
    "s_add_u32 m0, s82, 16384\n"
    "s_nop 0\n"
    "global_load_lds_dwordx4 v20, s[36:37]\n"
    "s_add_u32 m0, s82, 17408\n"
    "s_nop 0\n"
    "global_load_lds_dwordx4 v21, s[36:37]\n"
    "s_add_u32 m0, s82, 18432\n"
    "s_nop 0\n"
    "global_load_lds_dwordx4 v22, s[36:37]\n"
    "s_add_u32 m0, s82, 19456\n"
    "s_nop 0\n"
    "global_load_lds_dwordx4 v23, s[36:37]\n"
    "s_add_u32 s36, s36, 524288\n"
    "s_addc_u32 s37, s37, 0\n"
    "s_add_u32 m0, s82, 32768\n"
    "s_nop 0\n"
    "global_load_lds_dwordx4 v20, s[36:37]\n"
    "s_add_u32 m0, s82, 33792\n"
    "s_nop 0\n"
    "global_load_lds_dwordx4 v21, s[36:37]\n"
    "s_add_u32 m0, s82, 34816\n"
    "s_nop 0\n"
    "global_load_lds_dwordx4 v22, s[36:37]\n"
    "s_add_u32 m0, s82, 35840\n"
    "s_nop 0\n"
    "global_load_lds_dwordx4 v23, s[36:37]\n"
    "s_add_u32 s36, s36, 524288\n"
    "s_addc_u32 s37, s37, 0\n"
    "s_add_u32 m0, s82, 49152\n"
    "s_nop 0\n"
    "global_load_lds_dwordx4 v20, s[36:37]\n"
    "s_add_u32 m0, s82, 50176\n"
    "s_nop 0\n"
    "global_load_lds_dwordx4 v21, s[36:37]\n"
    "s_add_u32 m0, s82, 51200\n"
    "s_nop 0\n"
    "global_load_lds_dwordx4 v22, s[36:37]\n"
    "s_add_u32 m0, s82, 52224\n"
    "s_nop 0\n"
    "global_load_lds_dwordx4 v23, s[36:37]\n"
    "s_add_u32 s36, s36, 524288\n"
    "s_addc_u32 s37, s37, 0\n"
    "s_add_u32 m0, s82, 65536\n"
    "s_nop 0\n"
    "global_load_lds_dwordx4 v20, s[38:39]\n"
    "s_add_u32 m0, s82, 66560\n"
    "s_nop 0\n"
    "global_load_lds_dwordx4 v21, s[38:39]\n"
    "s_add_u32 m0, s82, 67584\n"
    "s_nop 0\n"
    "global_load_lds_dwordx4 v22, s[38:39]\n"
    "s_add_u32 m0, s82, 68608\n"
    "s_nop 0\n"
    "global_load_lds_dwordx4 v23, s[38:39]\n"
    "s_add_u32 s38, s38, 524288\n"
    "s_addc_u32 s39, s39, 0\n"
    "s_add_u32 m0, s82, 81920\n"
    "s_nop 0\n"
    "global_load_lds_dwordx4 v20, s[38:39]\n"
    "s_add_u32 m0, s82, 82944\n"
    "s_nop 0\n"
    "global_load_lds_dwordx4 v21, s[38:39]\n"
    "s_add_u32 m0, s82, 83968\n"
    "s_nop 0\n"
    "global_load_lds_dwordx4 v22, s[38:39]\n"
    "s_add_u32 m0, s82, 84992\n"
    "s_nop 0\n"
    "global_load_lds_dwordx4 v23, s[38:39]\n"
    "s_add_u32 s38, s38, 524288\n"
    "s_addc_u32 s39, s39, 0\n"
    "s_add_u32 m0, s82, 98304\n"
    "s_nop 0\n"
    "global_load_lds_dwordx4 v20, s[38:39]\n"
    "s_add_u32 m0, s82, 99328\n"
    "s_nop 0\n"
    "global_load_lds_dwordx4 v21, s[38:39]\n"
    "s_add_u32 m0, s82, 100352\n"
    "s_nop 0\n"
    "global_load_lds_dwordx4 v22, s[38:39]\n"
    "s_add_u32 m0, s82, 101376\n"
    "s_nop 0\n"
    "global_load_lds_dwordx4 v23, s[38:39]\n"
    "s_add_u32 s38, s38, 524288\n"
    "s_addc_u32 s39, s39, 0\n"
    "s_add_u32 m0, s82, 114688\n"
    "s_nop 0\n"
    "global_load_lds_dwordx4 v20, s[38:39]\n"
    "s_add_u32 m0, s82, 115712\n"
    "s_nop 0\n"
    "global_load_lds_dwordx4 v21, s[38:39]\n"
    "s_add_u32 m0, s82, 116736\n"
    "s_nop 0\n"
    "global_load_lds_dwordx4 v22, s[38:39]\n"
    "s_add_u32 m0, s82, 117760\n"
    "s_nop 0\n"
    "global_load_lds_dwordx4 v23, s[38:39]\n"
    "s_add_u32 s38, s38, 524288\n"
    "s_addc_u32 s39, s39, 0\n"
    "s_waitcnt vmcnt(16) lgkmcnt(0)\n"
    "s_barrier\n"
    "s_add_u32 m0, s83, 131072\n"
    "s_nop 0\n"
    "global_load_lds_dwordx4 v24, s[40:41]\n"
    "s_xor_b32 s81, s48, 0\n"
    "s_lshl_b32 s81, s81, 14\n"
    "s_add_u32 s81, s81, 0\n"
    "v_add_u32 v48, s81, v28\n"
    "v_add_u32 v49, s81, v29\n"
    "ds_read_b64_tr_b16 a[0:1], v48 offset:0\n"
    "ds_read_b64_tr_b16 a[8:9], v49 offset:0\n"
    "ds_read_b64_tr_b16 a[2:3], v48 offset:512\n"
    "ds_read_b64_tr_b16 a[10:11], v49 offset:512\n"
    "s_add_u32 m0, s83, 132096\n"
    "s_nop 0\n"
    "global_load_lds_dwordx4 v25, s[40:41]\n"
    "s_add_u32 s40, s40, 524288\n"
    "s_addc_u32 s41, s41, 0\n"
    "ds_read_b64_tr_b16 a[4:5], v48 offset:8192\n"
    "ds_read_b64_tr_b16 a[12:13], v49 offset:8192\n"
    "ds_read_b64_tr_b16 a[6:7], v48 offset:8704\n"
    "ds_read_b64_tr_b16 a[14:15], v49 offset:8704\n"
    "s_add_u32 m0, s83, 139264\n"
    "s_nop 0\n"
    "global_load_lds_dwordx4 v24, s[40:41]\n"
    "s_xor_b32 s81, s48, 1\n"
    "s_lshl_b32 s81, s81, 14\n"
    "s_add_u32 s81, s81, 0\n"
    "v_add_u32 v50, s81, v28\n"
    "v_add_u32 v51, s81, v29\n"
    "ds_read_b64_tr_b16 a[16:17], v50 offset:0\n"
    "ds_read_b64_tr_b16 a[24:25], v51 offset:0\n"
    "ds_read_b64_tr_b16 a[18:19], v50 offset:512\n"
    "ds_read_b64_tr_b16 a[26:27], v51 offset:512\n"
    "s_add_u32 m0, s83, 140288\n"
    "s_nop 0\n"
    "global_load_lds_dwordx4 v25, s[40:41]\n"
    "s_add_u32 s40, s40, 524288\n"
    "s_addc_u32 s41, s41, 0\n"
    "ds_read_b64_tr_b16 a[20:21], v50 offset:8192\n"
    "ds_read_b64_tr_b16 a[28:29], v51 offset:8192\n"
    "ds_read_b64_tr_b16 a[22:23], v50 offset:8704\n"
    "ds_read_b64_tr_b16 a[30:31], v51 offset:8704\n"
    "s_add_u32 m0, s83, 147456\n"
    "s_nop 0\n"
    "global_load_lds_dwordx4 v24, s[40:41]\n"
    "s_xor_b32 s81, s48, 2\n"
    "s_lshl_b32 s81, s81, 14\n"
    "s_add_u32 s81, s81, 0\n"
    "v_add_u32 v48, s81, v28\n"
    "v_add_u32 v49, s81, v29\n"
    "ds_read_b64_tr_b16 a[32:33], v48 offset:0\n"
    "ds_read_b64_tr_b16 a[40:41], v49 offset:0\n"
    "ds_read_b64_tr_b16 a[34:35], v48 offset:512\n"
    "ds_read_b64_tr_b16 a[42:43], v49 offset:512\n"
    "s_add_u32 m0, s83, 148480\n"
    "s_nop 0\n"
    "global_load_lds_dwordx4 v25, s[40:41]\n"
    "s_add_u32 s40, s40, 524288\n"
    "s_addc_u32 s41, s41, 0\n"
    "ds_read_b64_tr_b16 a[36:37], v48 offset:8192\n"
    "ds_read_b64_tr_b16 a[44:45], v49 offset:8192\n"
    "ds_read_b64_tr_b16 a[38:39], v48 offset:8704\n"
    "ds_read_b64_tr_b16 a[46:47], v49 offset:8704\n"
    "s_add_u32 m0, s83, 155648\n"
    "s_nop 0\n"
    "global_load_lds_dwordx4 v24, s[40:41]\n"
    "s_xor_b32 s81, s48, 3\n"
    "s_lshl_b32 s81, s81, 14\n"
    "s_add_u32 s81, s81, 0\n"
    "v_add_u32 v50, s81, v28\n"
    "v_add_u32 v51, s81, v29\n"
    "ds_read_b64_tr_b16 a[48:49], v50 offset:0\n"
    "ds_read_b64_tr_b16 a[56:57], v51 offset:0\n"
    "ds_read_b64_tr_b16 a[50:51], v50 offset:512\n"
    "ds_read_b64_tr_b16 a[58:59], v51 offset:512\n"
    "s_add_u32 m0, s83, 156672\n"
    "s_nop 0\n"
    "global_load_lds_dwordx4 v25, s[40:41]\n"
    "s_add_u32 s40, s40, 524288\n"
    "s_addc_u32 s41, s41, 0\n"
    "ds_read_b64_tr_b16 a[52:53], v50 offset:8192\n"
    "ds_read_b64_tr_b16 a[60:61], v51 offset:8192\n"
    "ds_read_b64_tr_b16 a[54:55], v50 offset:8704\n"
    "ds_read_b64_tr_b16 a[62:63], v51 offset:8704\n"
    "s_waitcnt vmcnt(8) lgkmcnt(0)\n"
    "s_barrier\n"
    "s_add_u32 m0, s82, 0\n"
    "s_nop 0\n"
    "global_load_lds_dwordx4 v20, s[36:37]\n"
    "s_xor_b32 s81, s48, 0\n"
    "s_lshl_b32 s81, s81, 14\n"
    "s_add_u32 s81, s81, 65536\n"
    "v_add_u32 v48, s81, v28\n"
    "v_add_u32 v49, s81, v29\n"
    "ds_read_b64_tr_b16 a[240:241], v48 offset:0\n"
    "ds_read_b64_tr_b16 a[160:161], v49 offset:0\n"
    "s_add_u32 m0, s82, 1024\n"
    "s_nop 0\n"
    "global_load_lds_dwordx4 v21, s[36:37]\n"
    "ds_read_b64_tr_b16 a[242:243], v48 offset:512\n"
    "ds_read_b64_tr_b16 a[162:163], v49 offset:512\n"
    "s_add_u32 m0, s82, 2048\n"
    "s_nop 0\n"
    "global_load_lds_dwordx4 v22, s[36:37]\n"
    "ds_read_b64_tr_b16 a[244:245], v48 offset:8192\n"
    "ds_read_b64_tr_b16 a[164:165], v49 offset:8192\n"
    "s_add_u32 m0, s82, 3072\n"
    "s_nop 0\n"
    "global_load_lds_dwordx4 v23, s[36:37]\n"
    "s_add_u32 s36, s36, 524288\n"
    "s_addc_u32 s37, s37, 0\n"
    "ds_read_b64_tr_b16 a[246:247], v48 offset:8704\n"
    "ds_read_b64_tr_b16 a[166:167], v49 offset:8704\n"
    "s_add_u32 m0, s82, 16384\n"
    "s_nop 0\n"
    "global_load_lds_dwordx4 v20, s[36:37]\n"
    "s_xor_b32 s81, s48, 1\n"
    "s_lshl_b32 s81, s81, 14\n"
    "s_add_u32 s81, s81, 65536\n"
    "v_add_u32 v50, s81, v28\n"
    "v_add_u32 v51, s81, v29\n"
    "ds_read_b64_tr_b16 a[248:249], v50 offset:0\n"
    "ds_read_b64_tr_b16 a[168:169], v51 offset:0\n"
    "s_add_u32 m0, s82, 17408\n"
    "s_nop 0\n"
    "global_load_lds_dwordx4 v21, s[36:37]\n"
    "ds_read_b64_tr_b16 a[250:251], v50 offset:512\n"
    "ds_read_b64_tr_b16 a[170:171], v51 offset:512\n"
    "s_add_u32 m0, s82, 18432\n"
    "s_nop 0\n"
    "global_load_lds_dwordx4 v22, s[36:37]\n"
    "ds_read_b64_tr_b16 a[252:253], v50 offset:8192\n"
    "ds_read_b64_tr_b16 a[172:173], v51 offset:8192\n"
    "s_add_u32 m0, s82, 19456\n"
    "s_nop 0\n"
    "global_load_lds_dwordx4 v23, s[36:37]\n"
    "s_add_u32 s36, s36, 524288\n"
    "s_addc_u32 s37, s37, 0\n"
    "ds_read_b64_tr_b16 a[254:255], v50 offset:8704\n"
    "ds_read_b64_tr_b16 a[174:175], v51 offset:8704\n"
    "s_add_u32 m0, s82, 32768\n"
    "s_nop 0\n"
    "global_load_lds_dwordx4 v20, s[36:37]\n"
    "s_xor_b32 s81, s48, 2\n"
    "s_lshl_b32 s81, s81, 14\n"
    "s_add_u32 s81, s81, 65536\n"
    "v_add_u32 v48, s81, v28\n"
    "v_add_u32 v49, s81, v29\n"
    "ds_read_b64_tr_b16 v[112:113], v48 offset:0\n"
    "ds_read_b64_tr_b16 a[176:177], v49 offset:0\n"
    "s_add_u32 m0, s82, 33792\n"
    "s_nop 0\n"
    "global_load_lds_dwordx4 v21, s[36:37]\n"
    "ds_read_b64_tr_b16 v[114:115], v48 offset:512\n"
    "ds_read_b64_tr_b16 a[178:179], v49 offset:512\n"
    "s_add_u32 m0, s82, 34816\n"
    "s_nop 0\n"
    "global_load_lds_dwordx4 v22, s[36:37]\n"
    "ds_read_b64_tr_b16 v[116:117], v48 offset:8192\n"
    "ds_read_b64_tr_b16 a[180:181], v49 offset:8192\n"
    "s_add_u32 m0, s82, 35840\n"
    "s_nop 0\n"
    "global_load_lds_dwordx4 v23, s[36:37]\n"
    "s_add_u32 s36, s36, 524288\n"
    "s_addc_u32 s37, s37, 0\n"
    "ds_read_b64_tr_b16 v[118:119], v48 offset:8704\n"
    "ds_read_b64_tr_b16 a[182:183], v49 offset:8704\n"
    "s_add_u32 m0, s82, 49152\n"
    "s_nop 0\n"
    "global_load_lds_dwordx4 v20, s[36:37]\n"
    "s_xor_b32 s81, s48, 3\n"
    "s_lshl_b32 s81, s81, 14\n"
    "s_add_u32 s81, s81, 65536\n"
    "v_add_u32 v50, s81, v28\n"
    "v_add_u32 v51, s81, v29\n"
    "ds_read_b64_tr_b16 v[120:121], v50 offset:0\n"
    "ds_read_b64_tr_b16 a[184:185], v51 offset:0\n"
    "s_add_u32 m0, s82, 50176\n"
    "s_nop 0\n"
    "global_load_lds_dwordx4 v21, s[36:37]\n"
    "ds_read_b64_tr_b16 v[122:123], v50 offset:512\n"
    "ds_read_b64_tr_b16 a[186:187], v51 offset:512\n"
    "s_add_u32 m0, s82, 51200\n"
    "s_nop 0\n"
    "global_load_lds_dwordx4 v22, s[36:37]\n"
    "ds_read_b64_tr_b16 v[124:125], v50 offset:8192\n"
    "ds_read_b64_tr_b16 a[188:189], v51 offset:8192\n"
    "s_add_u32 m0, s82, 52224\n"
    "s_nop 0\n"
    "global_load_lds_dwordx4 v23, s[36:37]\n"
    "s_add_u32 s36, s36, 524288\n"
    "s_addc_u32 s37, s37, 0\n"
    "ds_read_b64_tr_b16 v[126:127], v50 offset:8704\n"
    "ds_read_b64_tr_b16 a[190:191], v51 offset:8704\n"
    "s_waitcnt vmcnt(16) lgkmcnt(0)\n"
    "s_barrier\n"
    "s_add_u32 m0, s82, 65536\n"
    "s_nop 0\n"
    "global_load_lds_dwordx4 v20, s[38:39]\n"
    "s_xor_b32 s81, s48, 0\n"
    "s_lshl_b32 s81, s81, 13\n"
    "s_add_u32 s81, s81, 131072\n"
    "v_add_u32 v48, s81, v30\n"
    "ds_read_b64_tr_b16 v[176:177], v48 offset:0\n"
    "s_add_u32 m0, s82, 66560\n"
    "s_nop 0\n"
    "global_load_lds_dwordx4 v21, s[38:39]\n"
    "ds_read_b64_tr_b16 v[178:179], v48 offset:256\n"
    "s_add_u32 m0, s82, 67584\n"
    "s_nop 0\n"
    "global_load_lds_dwordx4 v22, s[38:39]\n"
    "ds_read_b64_tr_b16 v[180:181], v48 offset:4096\n"
    "s_add_u32 m0, s82, 68608\n"
    "s_nop 0\n"
    "global_load_lds_dwordx4 v23, s[38:39]\n"
    "s_add_u32 s38, s38, 524288\n"
    "s_addc_u32 s39, s39, 0\n"
    "ds_read_b64_tr_b16 v[182:183], v48 offset:4352\n"
    "s_add_u32 m0, s82, 81920\n"
    "s_nop 0\n"
    "global_load_lds_dwordx4 v20, s[38:39]\n"
    "s_xor_b32 s81, s48, 1\n"
    "s_lshl_b32 s81, s81, 13\n"
    "s_add_u32 s81, s81, 131072\n"
    "v_add_u32 v50, s81, v30\n"
    "ds_read_b64_tr_b16 v[184:185], v50 offset:0\n"
    "s_add_u32 m0, s82, 82944\n"
    "s_nop 0\n"
    "global_load_lds_dwordx4 v21, s[38:39]\n"
    "ds_read_b64_tr_b16 v[186:187], v50 offset:256\n"
    "s_add_u32 m0, s82, 83968\n"
    "s_nop 0\n"
    "global_load_lds_dwordx4 v22, s[38:39]\n"
    "ds_read_b64_tr_b16 v[188:189], v50 offset:4096\n"
    "s_add_u32 m0, s82, 84992\n"
    "s_nop 0\n"
    "global_load_lds_dwordx4 v23, s[38:39]\n"
    "s_add_u32 s38, s38, 524288\n"
    "s_addc_u32 s39, s39, 0\n"
    "ds_read_b64_tr_b16 v[190:191], v50 offset:4352\n"
    "s_add_u32 m0, s82, 98304\n"
    "s_nop 0\n"
    "global_load_lds_dwordx4 v20, s[38:39]\n"
    "s_xor_b32 s81, s48, 2\n"
    "s_lshl_b32 s81, s81, 13\n"
    "s_add_u32 s81, s81, 131072\n"
    "v_add_u32 v48, s81, v30\n"
    "ds_read_b64_tr_b16 v[192:193], v48 offset:0\n"
    "s_add_u32 m0, s82, 99328\n"
    "s_nop 0\n"
    "global_load_lds_dwordx4 v21, s[38:39]\n"
    "ds_read_b64_tr_b16 v[194:195], v48 offset:256\n"
    "s_add_u32 m0, s82, 100352\n"
    "s_nop 0\n"
    "global_load_lds_dwordx4 v22, s[38:39]\n"
    "ds_read_b64_tr_b16 v[196:197], v48 offset:4096\n"
    "s_add_u32 m0, s82, 101376\n"
    "s_nop 0\n"
    "global_load_lds_dwordx4 v23, s[38:39]\n"
    "s_add_u32 s38, s38, 524288\n"
    "s_addc_u32 s39, s39, 0\n"
    "ds_read_b64_tr_b16 v[198:199], v48 offset:4352\n"
    "s_add_u32 m0, s82, 114688\n"
    "s_nop 0\n"
    "global_load_lds_dwordx4 v20, s[38:39]\n"
    "s_xor_b32 s81, s48, 3\n"
    "s_lshl_b32 s81, s81, 13\n"
    "s_add_u32 s81, s81, 131072\n"
    "v_add_u32 v50, s81, v30\n"
    "ds_read_b64_tr_b16 v[200:201], v50 offset:0\n"
    "s_add_u32 m0, s82, 115712\n"
    "s_nop 0\n"
    "global_load_lds_dwordx4 v21, s[38:39]\n"
    "ds_read_b64_tr_b16 v[202:203], v50 offset:256\n"
    "s_add_u32 m0, s82, 116736\n"
    "s_nop 0\n"
    "global_load_lds_dwordx4 v22, s[38:39]\n"
    "ds_read_b64_tr_b16 v[204:205], v50 offset:4096\n"
    "s_add_u32 m0, s82, 117760\n"
    "s_nop 0\n"
    "global_load_lds_dwordx4 v23, s[38:39]\n"
    "s_add_u32 s38, s38, 524288\n"
    "s_addc_u32 s39, s39, 0\n"
    "ds_read_b64_tr_b16 v[206:207], v50 offset:4352\n"
    "s_waitcnt vmcnt(16) lgkmcnt(0)\n"
    "s_barrier\n"
    "s_add_u32 m0, s83, 131072\n"
    "s_nop 0\n"
    "global_load_lds_dwordx4 v24, s[40:41]\n"
    "s_xor_b32 s81, s48, 0\n"
    "s_lshl_b32 s81, s81, 14\n"
    "s_add_u32 s81, s81, 0\n"
    "v_add_u32 v48, s81, v28\n"
    "v_add_u32 v49, s81, v29\n"
    "ds_read_b64_tr_b16 a[64:65], v48 offset:0\n"
    "ds_read_b64_tr_b16 a[72:73], v49 offset:0\n"
    "ds_read_b64_tr_b16 a[66:67], v48 offset:512\n"
    "ds_read_b64_tr_b16 a[74:75], v49 offset:512\n"
    "s_add_u32 m0, s83, 132096\n"
    "s_nop 0\n"
    "global_load_lds_dwordx4 v25, s[40:41]\n"
    "s_add_u32 s40, s40, 524288\n"
    "s_addc_u32 s41, s41, 0\n"
    "ds_read_b64_tr_b16 a[68:69], v48 offset:8192\n"
    "ds_read_b64_tr_b16 a[76:77], v49 offset:8192\n"
    "ds_read_b64_tr_b16 a[70:71], v48 offset:8704\n"
    "ds_read_b64_tr_b16 a[78:79], v49 offset:8704\n"
    "s_add_u32 m0, s83, 139264\n"
    "s_nop 0\n"
    "global_load_lds_dwordx4 v24, s[40:41]\n"
    "s_xor_b32 s81, s48, 1\n"
    "s_lshl_b32 s81, s81, 14\n"
    "s_add_u32 s81, s81, 0\n"
    "v_add_u32 v50, s81, v28\n"
    "v_add_u32 v51, s81, v29\n"
    "ds_read_b64_tr_b16 a[80:81], v50 offset:0\n"
    "ds_read_b64_tr_b16 a[88:89], v51 offset:0\n"
    "ds_read_b64_tr_b16 a[82:83], v50 offset:512\n"
    "ds_read_b64_tr_b16 a[90:91], v51 offset:512\n"
    "s_add_u32 m0, s83, 140288\n"
    "s_nop 0\n"
    "global_load_lds_dwordx4 v25, s[40:41]\n"
    "s_add_u32 s40, s40, 524288\n"
    "s_addc_u32 s41, s41, 0\n"
    "ds_read_b64_tr_b16 a[84:85], v50 offset:8192\n"
    "ds_read_b64_tr_b16 a[92:93], v51 offset:8192\n"
    "ds_read_b64_tr_b16 a[86:87], v50 offset:8704\n"
    "ds_read_b64_tr_b16 a[94:95], v51 offset:8704\n"
    "s_add_u32 m0, s83, 147456\n"
    "s_nop 0\n"
    "global_load_lds_dwordx4 v24, s[40:41]\n"
    "s_xor_b32 s81, s48, 2\n"
    "s_lshl_b32 s81, s81, 14\n"
    "s_add_u32 s81, s81, 0\n"
    "v_add_u32 v48, s81, v28\n"
    "v_add_u32 v49, s81, v29\n"
    "ds_read_b64_tr_b16 a[96:97], v48 offset:0\n"
    "ds_read_b64_tr_b16 a[104:105], v49 offset:0\n"
    "ds_read_b64_tr_b16 a[98:99], v48 offset:512\n"
    "ds_read_b64_tr_b16 a[106:107], v49 offset:512\n"
    "s_add_u32 m0, s83, 148480\n"
    "s_nop 0\n"
    "global_load_lds_dwordx4 v25, s[40:41]\n"
    "s_add_u32 s40, s40, 524288\n"
    "s_addc_u32 s41, s41, 0\n"
    "ds_read_b64_tr_b16 a[100:101], v48 offset:8192\n"
    "ds_read_b64_tr_b16 a[108:109], v49 offset:8192\n"
    "ds_read_b64_tr_b16 a[102:103], v48 offset:8704\n"
    "ds_read_b64_tr_b16 a[110:111], v49 offset:8704\n"
    "s_add_u32 m0, s83, 155648\n"
    "s_nop 0\n"
    "global_load_lds_dwordx4 v24, s[40:41]\n"
    "s_xor_b32 s81, s48, 3\n"
    "s_lshl_b32 s81, s81, 14\n"
    "s_add_u32 s81, s81, 0\n"
    "v_add_u32 v50, s81, v28\n"
    "v_add_u32 v51, s81, v29\n"
    "ds_read_b64_tr_b16 a[112:113], v50 offset:0\n"
    "ds_read_b64_tr_b16 a[120:121], v51 offset:0\n"
    "ds_read_b64_tr_b16 a[114:115], v50 offset:512\n"
    "ds_read_b64_tr_b16 a[122:123], v51 offset:512\n"
    "s_add_u32 m0, s83, 156672\n"
    "s_nop 0\n"
    "global_load_lds_dwordx4 v25, s[40:41]\n"
    "s_add_u32 s40, s40, 524288\n"
    "s_addc_u32 s41, s41, 0\n"
    "ds_read_b64_tr_b16 a[116:117], v50 offset:8192\n"
    "ds_read_b64_tr_b16 a[124:125], v51 offset:8192\n"
    "ds_read_b64_tr_b16 a[118:119], v50 offset:8704\n"
    "ds_read_b64_tr_b16 a[126:127], v51 offset:8704\n"
    "s_waitcnt vmcnt(8) lgkmcnt(0)\n"
    "s_barrier\n"
    "s_add_u32 m0, s83, 0\n"
    "s_nop 0\n"
    "global_load_lds_dwordx4 v26, s[42:43]\n"
    "s_xor_b32 s81, s48, 0\n"
    "s_lshl_b32 s81, s81, 14\n"
    "s_add_u32 s81, s81, 65536\n"
    "v_add_u32 v48, s81, v28\n"
    "v_add_u32 v49, s81, v29\n"
    "ds_read_b64_tr_b16 v[128:129], v48 offset:0\n"
    "ds_read_b64_tr_b16 a[192:193], v49 offset:0\n"
    "s_add_u32 m0, s83, 1024\n"
    "s_nop 0\n"
    "global_load_lds_dwordx4 v27, s[42:43]\n"
    "s_add_u32 s42, s42, 256\n"
    "s_addc_u32 s43, s43, 0\n"
    "ds_read_b64_tr_b16 v[130:131], v48 offset:512\n"
    "ds_read_b64_tr_b16 a[194:195], v49 offset:512\n"
    "s_add_u32 m0, s83, 8192\n"
    "s_nop 0\n"
    "global_load_lds_dwordx4 v26, s[42:43]\n"
    "ds_read_b64_tr_b16 v[132:133], v48 offset:8192\n"
    "ds_read_b64_tr_b16 a[196:197], v49 offset:8192\n"
    "s_add_u32 m0, s83, 9216\n"
    "s_nop 0\n"
    "global_load_lds_dwordx4 v27, s[42:43]\n"
    "s_add_u32 s42, s42, 256\n"
    "s_addc_u32 s43, s43, 0\n"
    "ds_read_b64_tr_b16 v[134:135], v48 offset:8704\n"
    "ds_read_b64_tr_b16 a[198:199], v49 offset:8704\n"
    "s_add_u32 m0, s83, 16384\n"
    "s_nop 0\n"
    "global_load_lds_dwordx4 v26, s[42:43]\n"
    "s_xor_b32 s81, s48, 1\n"
    "s_lshl_b32 s81, s81, 14\n"
    "s_add_u32 s81, s81, 65536\n"
    "v_add_u32 v50, s81, v28\n"
    "v_add_u32 v51, s81, v29\n"
    "ds_read_b64_tr_b16 v[136:137], v50 offset:0\n"
    "ds_read_b64_tr_b16 a[200:201], v51 offset:0\n"
    "s_add_u32 m0, s83, 17408\n"
    "s_nop 0\n"
    "global_load_lds_dwordx4 v27, s[42:43]\n"
    "s_add_u32 s42, s42, 256\n"
    "s_addc_u32 s43, s43, 0\n"
    "ds_read_b64_tr_b16 v[138:139], v50 offset:512\n"
    "ds_read_b64_tr_b16 a[202:203], v51 offset:512\n"
    "s_add_u32 m0, s83, 24576\n"
    "s_nop 0\n"
    "global_load_lds_dwordx4 v26, s[42:43]\n"
    "ds_read_b64_tr_b16 v[140:141], v50 offset:8192\n"
    "ds_read_b64_tr_b16 a[204:205], v51 offset:8192\n"
    "s_add_u32 m0, s83, 25600\n"
    "s_nop 0\n"
    "global_load_lds_dwordx4 v27, s[42:43]\n"
    "s_add_u32 s42, s42, 256\n"
    "s_addc_u32 s43, s43, 0\n"
    "ds_read_b64_tr_b16 v[142:143], v50 offset:8704\n"
    "ds_read_b64_tr_b16 a[206:207], v51 offset:8704\n"
    "s_add_u32 m0, s83, 32768\n"
    "s_nop 0\n"
    "global_load_lds_dwordx4 v26, s[42:43]\n"
    "s_xor_b32 s81, s48, 2\n"
    "s_lshl_b32 s81, s81, 14\n"
    "s_add_u32 s81, s81, 65536\n"
    "v_add_u32 v48, s81, v28\n"
    "v_add_u32 v49, s81, v29\n"
    "ds_read_b64_tr_b16 v[144:145], v48 offset:0\n"
    "ds_read_b64_tr_b16 a[208:209], v49 offset:0\n"
    "s_add_u32 m0, s83, 33792\n"
    "s_nop 0\n"
    "global_load_lds_dwordx4 v27, s[42:43]\n"
    "s_add_u32 s42, s42, 39936\n"
    "s_addc_u32 s43, s43, 0\n"
    "ds_read_b64_tr_b16 v[146:147], v48 offset:512\n"
    "ds_read_b64_tr_b16 a[210:211], v49 offset:512\n"
    "s_add_u32 m0, s83, 40960\n"
    "s_nop 0\n"
    "global_load_lds_dwordx4 v26, s[42:43]\n"
    "ds_read_b64_tr_b16 v[148:149], v48 offset:8192\n"
    "ds_read_b64_tr_b16 a[212:213], v49 offset:8192\n"
    "s_add_u32 m0, s83, 41984\n"
    "s_nop 0\n"
    "global_load_lds_dwordx4 v27, s[42:43]\n"
    "s_add_u32 s42, s42, 256\n"
    "s_addc_u32 s43, s43, 0\n"
    "ds_read_b64_tr_b16 v[150:151], v48 offset:8704\n"
    "ds_read_b64_tr_b16 a[214:215], v49 offset:8704\n"
    "s_add_u32 m0, s83, 49152\n"
    "s_nop 0\n"
    "global_load_lds_dwordx4 v26, s[42:43]\n"
    "s_xor_b32 s81, s48, 3\n"
    "s_lshl_b32 s81, s81, 14\n"
    "s_add_u32 s81, s81, 65536\n"
    "v_add_u32 v50, s81, v28\n"
    "v_add_u32 v51, s81, v29\n"
    "ds_read_b64_tr_b16 v[152:153], v50 offset:0\n"
    "ds_read_b64_tr_b16 a[216:217], v51 offset:0\n"
    "s_add_u32 m0, s83, 50176\n"
    "s_nop 0\n"
    "global_load_lds_dwordx4 v27, s[42:43]\n"
    "s_add_u32 s42, s42, 256\n"
    "s_addc_u32 s43, s43, 0\n"
    "ds_read_b64_tr_b16 v[154:155], v50 offset:512\n"
    "ds_read_b64_tr_b16 a[218:219], v51 offset:512\n"
    "s_add_u32 m0, s83, 57344\n"
    "s_nop 0\n"
    "global_load_lds_dwordx4 v26, s[42:43]\n"
    "ds_read_b64_tr_b16 v[156:157], v50 offset:8192\n"
    "ds_read_b64_tr_b16 a[220:221], v51 offset:8192\n"
    "s_add_u32 m0, s83, 58368\n"
    "s_nop 0\n"
    "global_load_lds_dwordx4 v27, s[42:43]\n"
    "s_add_u32 s42, s42, 256\n"
    "s_addc_u32 s43, s43, 0\n"
    "ds_read_b64_tr_b16 v[158:159], v50 offset:8704\n"
    "ds_read_b64_tr_b16 a[222:223], v51 offset:8704\n"
    "s_waitcnt vmcnt(16) lgkmcnt(0)\n"
    "s_barrier\n"
    "s_add_u32 m0, s82, 81920\n"
    "s_nop 0\n"
    "global_load_lds_dwordx4 v20, s[36:37]\n"
    "s_xor_b32 s81, s48, 0\n"
    "s_lshl_b32 s81, s81, 13\n"
    "s_add_u32 s81, s81, 131072\n"
    "v_add_u32 v48, s81, v30\n"
    "ds_read_b64_tr_b16 v[208:209], v48 offset:0\n"
    "s_add_u32 m0, s82, 82944\n"
    "s_nop 0\n"
    "global_load_lds_dwordx4 v21, s[36:37]\n"
    "ds_read_b64_tr_b16 v[210:211], v48 offset:256\n"
    "s_add_u32 m0, s82, 83968\n"
    "s_nop 0\n"
    "global_load_lds_dwordx4 v22, s[36:37]\n"
    "ds_read_b64_tr_b16 v[212:213], v48 offset:4096\n"
    "s_add_u32 m0, s82, 84992\n"
    "s_nop 0\n"
    "global_load_lds_dwordx4 v23, s[36:37]\n"
    "s_add_u32 s36, s36, 524288\n"
    "s_addc_u32 s37, s37, 0\n"
    "ds_read_b64_tr_b16 v[214:215], v48 offset:4352\n"
    "s_add_u32 m0, s82, 98304\n"
    "s_nop 0\n"
    "global_load_lds_dwordx4 v20, s[36:37]\n"
    "s_xor_b32 s81, s48, 1\n"
    "s_lshl_b32 s81, s81, 13\n"
    "s_add_u32 s81, s81, 131072\n"
    "v_add_u32 v50, s81, v30\n"
    "ds_read_b64_tr_b16 v[216:217], v50 offset:0\n"
    "s_add_u32 m0, s82, 99328\n"
    "s_nop 0\n"
    "global_load_lds_dwordx4 v21, s[36:37]\n"
    "ds_read_b64_tr_b16 v[218:219], v50 offset:256\n"
    "s_add_u32 m0, s82, 100352\n"
    "s_nop 0\n"
    "global_load_lds_dwordx4 v22, s[36:37]\n"
    "ds_read_b64_tr_b16 v[220:221], v50 offset:4096\n"
    "s_add_u32 m0, s82, 101376\n"
    "s_nop 0\n"
    "global_load_lds_dwordx4 v23, s[36:37]\n"
    "s_add_u32 s36, s36, 524288\n"
    "s_addc_u32 s37, s37, 0\n"
    "ds_read_b64_tr_b16 v[222:223], v50 offset:4352\n"
    "s_add_u32 m0, s83, 114688\n"
    "s_nop 0\n"
    "global_load_lds_dwordx4 v24, s[40:41]\n"
    "s_xor_b32 s81, s48, 2\n"
    "s_lshl_b32 s81, s81, 13\n"
    "s_add_u32 s81, s81, 131072\n"
    "v_add_u32 v48, s81, v30\n"
    "ds_read_b64_tr_b16 v[224:225], v48 offset:0\n"
    "s_add_u32 m0, s83, 115712\n"
    "s_nop 0\n"
    "global_load_lds_dwordx4 v25, s[40:41]\n"
    "s_add_u32 s40, s40, 524288\n"
    "s_addc_u32 s41, s41, 0\n"
    "ds_read_b64_tr_b16 v[226:227], v48 offset:256\n"
    "s_add_u32 m0, s83, 122880\n"
    "s_nop 0\n"
    "global_load_lds_dwordx4 v24, s[40:41]\n"
    "ds_read_b64_tr_b16 v[228:229], v48 offset:4096\n"
    "s_add_u32 m0, s83, 123904\n"
    "s_nop 0\n"
    "global_load_lds_dwordx4 v25, s[40:41]\n"
    "s_add_u32 s40, s40, 524288\n"
    "s_addc_u32 s41, s41, 0\n"
    "ds_read_b64_tr_b16 v[230:231], v48 offset:4352\n"
    "s_add_u32 m0, s83, 65536\n"
    "s_nop 0\n"
    "global_load_lds_dwordx4 v26, s[42:43]\n"
    "s_xor_b32 s81, s48, 3\n"
    "s_lshl_b32 s81, s81, 13\n"
    "s_add_u32 s81, s81, 131072\n"
    "v_add_u32 v50, s81, v30\n"
    "ds_read_b64_tr_b16 v[232:233], v50 offset:0\n"
    "s_add_u32 m0, s83, 66560\n"
    "s_nop 0\n"
    "global_load_lds_dwordx4 v27, s[42:43]\n"
    "s_add_u32 s42, s42, 256\n"
    "s_addc_u32 s43, s43, 0\n"
    "ds_read_b64_tr_b16 v[234:235], v50 offset:256\n"
    "s_add_u32 m0, s83, 73728\n"
    "s_nop 0\n"
    "global_load_lds_dwordx4 v26, s[42:43]\n"
    "ds_read_b64_tr_b16 v[236:237], v50 offset:4096\n"
    "s_add_u32 m0, s83, 74752\n"
    "s_nop 0\n"
    "global_load_lds_dwordx4 v27, s[42:43]\n"
    "ds_read_b64_tr_b16 v[238:239], v50 offset:4352\n"
    "s_waitcnt vmcnt(4) lgkmcnt(0)\n"
    "s_barrier\n"
    "s_add_u32 m0, s82, 131072\n"
    "s_nop 0\n"
    "global_load_lds_dwordx4 v20, s[38:39]\n"
    "s_mov_b32 s81, 0\n"
    "s_lshl_b32 s81, s81, 14\n"
    "s_add_u32 s81, s81, 81920\n"
    "v_add_u32 v48, s81, v28\n"
    "v_add_u32 v49, s81, v29\n"
    "ds_read_b64_tr_b16 a[128:129], v48 offset:0\n"
    "ds_read_b64_tr_b16 a[136:137], v49 offset:0\n"
    "ds_read_b64_tr_b16 a[130:131], v48 offset:512\n"
    "s_add_u32 m0, s82, 132096\n"
    "s_nop 0\n"
    "global_load_lds_dwordx4 v21, s[38:39]\n"
    "ds_read_b64_tr_b16 a[138:139], v49 offset:512\n"
    "ds_read_b64_tr_b16 a[132:133], v48 offset:8192\n"
    "ds_read_b64_tr_b16 a[140:141], v49 offset:8192\n"
    "s_add_u32 m0, s82, 133120\n"
    "s_nop 0\n"
    "global_load_lds_dwordx4 v22, s[38:39]\n"
    "ds_read_b64_tr_b16 a[134:135], v48 offset:8704\n"
    "ds_read_b64_tr_b16 a[142:143], v49 offset:8704\n"
    "s_mov_b32 s81, 1\n"
    "s_lshl_b32 s81, s81, 14\n"
    "s_add_u32 s81, s81, 81920\n"
    "v_add_u32 v50, s81, v28\n"
    "v_add_u32 v51, s81, v29\n"
    "ds_read_b64_tr_b16 a[144:145], v50 offset:0\n"
    "s_add_u32 m0, s82, 134144\n"
    "s_nop 0\n"
    "global_load_lds_dwordx4 v23, s[38:39]\n"
    "s_add_u32 s38, s38, 524288\n"
    "s_addc_u32 s39, s39, 0\n"
    "ds_read_b64_tr_b16 a[152:153], v51 offset:0\n"
    "ds_read_b64_tr_b16 a[146:147], v50 offset:512\n"
    "ds_read_b64_tr_b16 a[154:155], v51 offset:512\n"
    "s_add_u32 m0, s82, 147456\n"
    "s_nop 0\n"
    "global_load_lds_dwordx4 v20, s[38:39]\n"
    "ds_read_b64_tr_b16 a[148:149], v50 offset:8192\n"
    "ds_read_b64_tr_b16 a[156:157], v51 offset:8192\n"
    "ds_read_b64_tr_b16 a[150:151], v50 offset:8704\n"
    "s_add_u32 m0, s82, 148480\n"
    "s_nop 0\n"
    "global_load_lds_dwordx4 v21, s[38:39]\n"
    "ds_read_b64_tr_b16 a[158:159], v51 offset:8704\n"
    "s_mov_b32 s81, 0\n"
    "s_lshl_b32 s81, s81, 13\n"
    "s_add_u32 s81, s81, 114688\n"
    "v_add_u32 v48, s81, v30\n"
    "ds_read_b64_tr_b16 v[240:241], v48 offset:0\n"
    "ds_read_b64_tr_b16 v[242:243], v48 offset:256\n"
    "s_add_u32 m0, s82, 149504\n"
    "s_nop 0\n"
    "global_load_lds_dwordx4 v22, s[38:39]\n"
    "ds_read_b64_tr_b16 v[244:245], v48 offset:4096\n"
    "ds_read_b64_tr_b16 v[246:247], v48 offset:4352\n"
    "s_mov_b32 s81, 1\n"
    "s_lshl_b32 s81, s81, 13\n"
    "s_add_u32 s81, s81, 114688\n"
    "v_add_u32 v50, s81, v30\n"
    "ds_read_b64_tr_b16 v[248:249], v50 offset:0\n"
    "s_add_u32 m0, s82, 150528\n"
    "s_nop 0\n"
    "global_load_lds_dwordx4 v23, s[38:39]\n"
    "s_add_u32 s38, s38, 524288\n"
    "s_addc_u32 s39, s39, 0\n"
    "ds_read_b64_tr_b16 v[250:251], v50 offset:256\n"
    "ds_read_b64_tr_b16 v[252:253], v50 offset:4096\n"
    "ds_read_b64_tr_b16 v[254:255], v50 offset:4352\n"
    "s_waitcnt vmcnt(0) lgkmcnt(0)\n"
    "s_barrier\n"
    "s_mov_b32 s81, 0\n"
    "s_lshl_b32 s81, s81, 14\n"
    "s_add_u32 s81, s81, 131072\n"
    "v_add_u32 v48, s81, v28\n"
    "v_add_u32 v49, s81, v29\n"
    "ds_read_b64_tr_b16 v[160:161], v48 offset:0\n"
    "ds_read_b64_tr_b16 a[224:225], v49 offset:0\n"
    "ds_read_b64_tr_b16 v[162:163], v48 offset:512\n"
    "ds_read_b64_tr_b16 a[226:227], v49 offset:512\n"
    "ds_read_b64_tr_b16 v[164:165], v48 offset:8192\n"
    "ds_read_b64_tr_b16 a[228:229], v49 offset:8192\n"
    "ds_read_b64_tr_b16 v[166:167], v48 offset:8704\n"
    "ds_read_b64_tr_b16 a[230:231], v49 offset:8704\n"
    "s_mov_b32 s81, 1\n"
    "s_lshl_b32 s81, s81, 14\n"
    "s_add_u32 s81, s81, 131072\n"
    "v_add_u32 v50, s81, v28\n"
    "v_add_u32 v51, s81, v29\n"
    "ds_read_b64_tr_b16 v[168:169], v50 offset:0\n"
    "ds_read_b64_tr_b16 a[232:233], v51 offset:0\n"
    "ds_read_b64_tr_b16 v[170:171], v50 offset:512\n"
    "ds_read_b64_tr_b16 a[234:235], v51 offset:512\n"
    "ds_read_b64_tr_b16 v[172:173], v50 offset:8192\n"
    "ds_read_b64_tr_b16 a[236:237], v51 offset:8192\n"
    "ds_read_b64_tr_b16 v[174:175], v50 offset:8704\n"
    "ds_read_b64_tr_b16 a[238:239], v51 offset:8704\n"
    "s_waitcnt lgkmcnt(0)\n"
    "s_barrier\n"
    "ds_write_b128 v8, a[240:243] offset:0\n"
    "ds_write_b128 v8, a[244:247] offset:1024\n"
    "ds_write_b128 v8, a[248:251] offset:2048\n"
    "ds_write_b128 v8, a[252:255] offset:3072\n"
    "ds_write_b128 v8, v[112:115] offset:4096\n"
    "ds_write_b128 v8, v[116:119] offset:5120\n"
    "ds_write_b128 v8, v[120:123] offset:6144\n"
    "ds_write_b128 v8, v[124:127] offset:7168\n"
    "ds_write_b128 v8, v[128:131] offset:8192\n"
    "ds_write_b128 v8, v[132:135] offset:9216\n"
    "ds_write_b128 v8, v[136:139] offset:10240\n"
    "ds_write_b128 v8, v[140:143] offset:11264\n"
    "ds_write_b128 v8, v[144:147] offset:12288\n"
    "ds_write_b128 v8, v[148:151] offset:13312\n"
    "ds_write_b128 v8, v[152:155] offset:14336\n"
    "ds_write_b128 v8, v[156:159] offset:15360\n"
    "ds_write_b128 v8, v[160:163] offset:16384\n"
    "ds_write_b128 v8, v[164:167] offset:17408\n"
    "ds_write_b128 v8, v[168:171] offset:18432\n"
    "ds_write_b128 v8, v[172:175] offset:19456\n"
    "s_waitcnt lgkmcnt(0)\n"
    "ds_read_b64_tr_b16 a[240:241], v32 offset:0\n"
    "ds_read_b64_tr_b16 a[242:243], v32 offset:512\n"
    "ds_read_b64_tr_b16 a[244:245], v32 offset:2048\n"
    "ds_read_b64_tr_b16 a[246:247], v32 offset:2560\n"
    "ds_read_b64_tr_b16 a[248:249], v33 offset:0\n"
    "ds_read_b64_tr_b16 a[250:251], v33 offset:512\n"
    "ds_read_b64_tr_b16 a[252:253], v33 offset:2048\n"
    "ds_read_b64_tr_b16 a[254:255], v33 offset:2560\n"
    "ds_read_b64_tr_b16 v[112:113], v34 offset:0\n"
    "ds_read_b64_tr_b16 v[114:115], v34 offset:512\n"
    "ds_read_b64_tr_b16 v[116:117], v34 offset:2048\n"
    "ds_read_b64_tr_b16 v[118:119], v34 offset:2560\n"
    "ds_read_b64_tr_b16 v[120:121], v35 offset:0\n"
    "ds_read_b64_tr_b16 v[122:123], v35 offset:512\n"
    "ds_read_b64_tr_b16 v[124:125], v35 offset:2048\n"
    "ds_read_b64_tr_b16 v[126:127], v35 offset:2560\n"
    "ds_read_b64_tr_b16 v[128:129], v36 offset:0\n"
    "ds_read_b64_tr_b16 v[130:131], v36 offset:512\n"
    "ds_read_b64_tr_b16 v[132:133], v36 offset:2048\n"
    "ds_read_b64_tr_b16 v[134:135], v36 offset:2560\n"
    "ds_read_b64_tr_b16 v[136:137], v37 offset:0\n"
    "ds_read_b64_tr_b16 v[138:139], v37 offset:512\n"
    "ds_read_b64_tr_b16 v[140:141], v37 offset:2048\n"
    "ds_read_b64_tr_b16 v[142:143], v37 offset:2560\n"
    "ds_read_b64_tr_b16 v[144:145], v38 offset:0\n"
    "ds_read_b64_tr_b16 v[146:147], v38 offset:512\n"
    "ds_read_b64_tr_b16 v[148:149], v38 offset:2048\n"
    "ds_read_b64_tr_b16 v[150:151], v38 offset:2560\n"
    "ds_read_b64_tr_b16 v[152:153], v39 offset:0\n"
    "ds_read_b64_tr_b16 v[154:155], v39 offset:512\n"
    "ds_read_b64_tr_b16 v[156:157], v39 offset:2048\n"
    "ds_read_b64_tr_b16 v[158:159], v39 offset:2560\n"
    "ds_read_b64_tr_b16 v[160:161], v40 offset:0\n"
    "ds_read_b64_tr_b16 v[162:163], v40 offset:512\n"
    "ds_read_b64_tr_b16 v[164:165], v40 offset:2048\n"
    "ds_read_b64_tr_b16 v[166:167], v40 offset:2560\n"
    "ds_read_b64_tr_b16 v[168:169], v41 offset:0\n"
    "ds_read_b64_tr_b16 v[170:171], v41 offset:512\n"
    "ds_read_b64_tr_b16 v[172:173], v41 offset:2048\n"
    "ds_read_b64_tr_b16 v[174:175], v41 offset:2560\n"
    "s_waitcnt lgkmcnt(0)\n"
    "s_barrier\n"
    "v_mov_b32 v16, 0x3f803f80\n"
    "v_mov_b32 v17, 0x3f803f80\n"
    "v_mov_b32 v18, 0x3f803f80\n"
    "v_mov_b32 v19, 0x3f803f80\n"
    "s_mov_b32 s88, 0x33333333\n"
    "s_mov_b32 s89, 0x33333333\n"
    "s_mov_b32 s94, 0x3e0293ee\n"
    "s_mov_b32 s95, 0x3e0293ee\n"
    "s_mov_b32 s80, 0\n"
    "s_mov_b32 s93, 0\n"
    "s_mov_b32 s76, 0\n"
    "s_and_b32 s34, s49, 1\n"
    "s_lshl_b32 s34, s34, 12\n"
    "s_add_u32 s34, s34, 16384\n"
    "s_mov_b32 s90, 0\n"
    "s_and_b32 s82, s46, 0x1ffff\n"
    "gate_%=:\n"
    "buffer_load_dword v20, off, s[64:67], s82 sc1\n"
    "s_waitcnt vmcnt(0)\n"
    "v_cmp_eq_u32 vcc, v20, v5\n"
    "s_cbranch_vccnz gateok_%=\n"
    "s_sleep 2\n"
    "s_add_u32 s90, s90, 1\n"
    "s_or_b32 s81, s90, s80\n"
    "s_cmp_lt_u32 s81, 262144\n"
    "s_cbranch_scc1 gate_%=\n"
    "s_mov_b32 s80, 262144\n"
    "gateok_%=:\n"
    "s_lshr_b32 s82, s46, 17\n"
    "v_mov_b32 v21, 1\n"
    "buffer_store_dword v21, off, s[64:67], s82 sc1\n"
    "ini_%=:\n"
    "buffer_load_dword v20, off, s[64:67], 0 sc1\n"
    "s_waitcnt vmcnt(0)\n"
    "v_readfirstlane_b32 s82, v20\n"
    "s_or_b32 s81, s82, 1\n"
    "s_cmp_ge_u32 s81, s75\n"
    "s_cbranch_scc1 iniok_%=\n"
    "s_sleep 2\n"
    "s_add_u32 s90, s90, 1\n"
    "s_or_b32 s81, s90, s80\n"
    "s_cmp_lt_u32 s81, 262144\n"
    "s_cbranch_scc1 ini_%=\n"
    "s_mov_b32 s80, 262144\n"
    "iniok_%=:\n"
    "s_and_b32 s82, s82, 255\n"
    "s_cmp_gt_u32 s82, 16\n"
    "s_cselect_b32 s82, 8192, 0\n"
    "s_xor_b32 s77, s82, 8192\n"
    "s_xor_b32 s36, s82, 0x600000\n"
    "s_and_b32 s81, s75, 1\n"
    "s_lshl_b32 s81, s81, 13\n"
    "s_xor_b32 s81, s81, s82\n"
    "s_add_u32 s64, s64, s81\n"
    "s_xor_b32 s91, s74, s82\n"
    "s_sub_u32 s92, s91, 8192\n"
    "s_lshr_b32 s30, s47, 8\n"
    "s_and_b32 s47, s47, 255\n"
    "s_add_u32 s37, s36, 4096\n"
    "s_add_u32 s38, s36, 16384\n"
    "s_mov_b32 s74, s51\n"
    "s_mov_b32 s75, s50\n"
    "v_mov_b32 v60, 0xff81ff81\n"
    "v_mov_b32 v61, 0xff81ff81\n"
    "v_mov_b32 v62, 0xff81ff81\n"
    "v_mov_b32 v63, 0xff81ff81\n"
    "buffer_store_dwordx4 v[60:63], v9, s[52:55], s77 offen sc1\n"
    "buffer_store_dwordx4 v[60:63], v10, s[52:55], s77 offen sc1\n"
    "buffer_store_dwordx2 v[60:61], v11, s[52:55], s77 offen sc1\n"
    "s_mov_b32 s100, 1\n"
    "s_mov_b32 s86, -1\n"
    "s_mov_b32 s98, 90\n"
    "s_mov_b32 s101, 656\n"
    "s_lshr_b32 s39, s68, 16\n"
    "s_and_b32 s68, s68, 0xffff\n"
    "s_lshl_b32 s43, s73, 18\n"
    "s_mov_b32 s99, 0\n"
    "s_add_u32 s31, s76, 0\n"
    "s_min_u32 s31, s31, 63\n"
    "s_cmp_lt_u32 s31, s93\n"
    "s_cselect_b32 s42, 0, 1\n"
    "s_lshr_b32 s81, s31, 1\n"
    "s_add_u32 s81, s31, s81\n"
    "s_lshl_b32 s87, s81, 21\n"
    "s_xor_b32 s81, s87, s91\n"
    "s_lshr_b32 s81, s81, 8\n"
    "s_xor_b32 s81, s81, s91\n"
    "s_and_b32 s81, s81, 8192\n"
    "s_add_u32 s87, s87, s81\n"
    "s_add_u32 s40, s87, 4096\n"
    "s_add_u32 s41, s87, s34\n"
    "s_mov_b32 s90, 0\n"
    "kvpi_%=:\n"
    "buffer_load_dword v108, off, s[64:67], s69 offset:16 sc1\n"
    "buffer_load_dword v109, off, s[64:67], s69 offset:32 sc1\n"
    "s_waitcnt vmcnt(0)\n"
    "v_min_u32 v108, v108, v109\n"
    "s_nop 0\n"
    "v_readfirstlane_b32 s93, v108\n"
    "s_cmp_lt_u32 s31, s93\n"
    "s_cbranch_scc1 kvoki_%=\n"
    "s_sleep 2\n"
    "s_add_u32 s90, s90, 1\n"
    "s_or_b32 s81, s90, s80\n"
    "s_cmp_lt_u32 s81, 262144\n"
    "s_cbranch_scc1 kvpi_%=\n"
    "s_mov_b32 s80, 262144\n"
    "kvoki_%=:\n"
    "buffer_load_dwordx4 v[96:99], v4, s[56:59], s87 offen sc1\n"
    "buffer_load_dwordx4 v[100:103], v4, s[56:59], s40 offen sc1\n"
    "buffer_load_dwordx4 v[104:107], v4, s[56:59], s41 offen sc1\n"
    "s_waitcnt vmcnt(0)\n"
    "ds_read_b128 v[80:83], v8 offset:0\n"
    "ds_read_b128 v[84:87], v8 offset:1024\n"
    "ds_read_b128 v[88:91], v8 offset:2048\n"
    "ds_read_b128 v[92:95], v8 offset:3072\n"
    "ds_read_b128 v[44:47], v8 offset:6144\n"
    "ds_read_b128 v[48:51], v8 offset:7168\n"
    "ds_read_b128 v[52:55], v8 offset:8192\n"
    "ds_read_b128 v[56:59], v8 offset:9216\n"
    "v_mov_b64 v[60:61], 0\n"
    "v_mov_b64 v[62:63], 0\n"
    "v_mov_b64 v[64:65], 0\n"
    "v_mov_b64 v[66:67], 0\n"
    "v_mov_b64 v[68:69], 0\n"
    "v_mov_b64 v[70:71], 0\n"
    "v_mov_b64 v[72:73], 0\n"
    "v_mov_b64 v[74:75], 0\n"
    "v_mov_b64 v[76:77], 0\n"
    "v_mov_b64 v[78:79], 0\n"
    "v_or_b32 v108, s99, v4\n"
    "ds_write_b128 v108, v[96:99] offset:12288\n"
    "ds_write_b128 v108, v[100:103] offset:16384\n"
    "ds_write_b128 v108, v[104:107] offset:20480\n"
    "s_xor_b32 s99, s99, 12288\n"
    "ds_read_b128 v[96:99], v8 offset:4096\n"
    "ds_read_b128 v[100:103], v8 offset:5120\n"
    "s_add_u32 s31, s76, 1\n"
    "s_min_u32 s31, s31, 63\n"
    "s_cmp_lt_u32 s31, s93\n"
    "s_cselect_b32 s42, 0, 1\n"
    "s_lshr_b32 s81, s31, 1\n"
    "s_add_u32 s81, s31, s81\n"
    "s_lshl_b32 s87, s81, 21\n"
    "s_xor_b32 s81, s87, s91\n"
    "s_lshr_b32 s81, s81, 8\n"
    "s_xor_b32 s81, s81, s91\n"
    "s_and_b32 s81, s81, 8192\n"
    "s_add_u32 s87, s87, s81\n"
    "s_add_u32 s40, s87, 4096\n"
    "s_add_u32 s41, s87, s34\n"
    "v_xor_b32 v109, 1024, v4\n"
    "v_xor_b32 v110, 2048, v4\n"
    "v_xor_b32 v111, 3072, v4\n"
    "v_mov_b32 v104, s76\n"
    "v_mov_b32 v105, s100\n"
    "s_memrealtime s[96:97]\n"
    "s_memrealtime s[48:49]\n"
    "s_waitcnt lgkmcnt(0)\n"
    "s_lshl_b32 s46, s48, 18\n"
    "s_barrier\n"
    "loop_%=:\n"
    "buffer_load_dwordx4 v[20:23], v4, s[52:55], s36 offen sc1\n"
    "s_mov_b32 s81, s47\n"
    "dg0_%=:\n"
    "s_sub_u32 s81, s81, 1\n"
    "s_cbranch_scc0 dg0_%=\n"
    "buffer_load_dwordx4 v[36:39], v4, s[52:55], s37 offen sc1\n"
    "s_mov_b32 s81, s30\n"
    "dg1_%=:\n"
    "s_sub_u32 s81, s81, 1\n"
    "s_cbranch_scc0 dg1_%=\n"
    "buffer_load_dwordx2 v[40:41], v13, s[52:55], s38 offen sc1\n"
    "buffer_store_dwordx2 v[104:105], v12, s[64:67], 0 offen sc1\n"
    "s_waitcnt vmcnt(3)\n"
    "ret0_%=:\n"
    "ds_write_b128 v4, v[20:23] offset:49152\n"
    "v_max3_u32 v108, v20, v21, v22\n"
    "v_max_u32 v108, v108, v23\n"
    "v_smfmac_f32_16x16x64_bf16 v[64:67], v[20:23], v[80:87], v1\n"
    "v_cmp_eq_u32 vcc, -1, v108\n"
    "v_smfmac_f32_16x16x64_bf16 v[60:63], v[20:23], a[0:7], v1\n"
    "s_cbranch_vccnz fail0_%=\n"
    "s_waitcnt lgkmcnt(0)\n"
    "s_barrier\n"
    "ds_read_b128 v[24:27], v109 offset:49152\n"
    "ds_read_b128 v[28:31], v110 offset:49152\n"
    "v_smfmac_f32_16x16x64_bf16 v[68:71], v[20:23], a[8:15], v1\n"
    "ds_read_b128 v[32:35], v111 offset:49152\n"
    "v_smfmac_f32_16x16x64_bf16 v[72:75], v[20:23], a[160:167], v1\n"
    "ds_read_b128 v[80:83], v8 offset:10240\n"
    "v_smfmac_f32_16x16x64_bf16 v[76:79], v[20:23], v[176:183], v1\n"
    "ds_read_b128 v[84:87], v8 offset:11264\n"
    "s_waitcnt lgkmcnt(2)\n"
    "v_smfmac_f32_16x16x64_bf16 v[64:67], v[24:27], v[88:95], v1\n"
    "ds_read_b128 v[88:91], v8 offset:12288\n"
    "v_smfmac_f32_16x16x64_bf16 v[60:63], v[24:27], a[16:23], v1\n"
    "ds_read_b128 v[92:95], v8 offset:13312\n"
    "v_smfmac_f32_16x16x64_bf16 v[68:71], v[24:27], a[24:31], v1\n"
    "s_waitcnt vmcnt(1)\n"
    "v_smfmac_f32_16x16x64_bf16 v[72:75], v[24:27], a[168:175], v1\n"
    "ds_write_b128 v4, v[36:39] offset:53248\n"
    "v_smfmac_f32_16x16x64_bf16 v[64:67], v[28:31], v[96:103], v1\n"
    "ds_write_b64 v13, v[40:41] offset:57344\n"
    "v_smfmac_f32_16x16x64_bf16 v[60:63], v[28:31], a[32:39], v1\n"
    "v_max3_u32 v108, v36, v37, v38\n"
    "v_smfmac_f32_16x16x64_bf16 v[68:71], v[28:31], a[40:47], v1\n"
    "v_max3_u32 v108, v108, v39, v40\n"
    "ds_read_b128 v[96:99], v8 offset:14336\n"
    "v_smfmac_f32_16x16x64_bf16 v[72:75], v[28:31], a[176:183], v1\n"
    "v_max_u32 v108, v108, v41\n"
    "ds_read_b128 v[100:103], v8 offset:15360\n"
    "v_smfmac_f32_16x16x64_bf16 v[64:67], v[32:35], v[44:51], v1\n"
    "v_cmp_eq_u32 vcc, -1, v108\n"
    "v_smfmac_f32_16x16x64_bf16 v[60:63], v[32:35], a[48:55], v1\n"
    "s_cbranch_vccnz fail4_%=\n"
    "ret4_%=:\n"
    "v_smfmac_f32_16x16x64_bf16 v[64:67], v[36:39], v[52:59], v1\n"
    "s_waitcnt lgkmcnt(2)\n"
    "s_barrier\n"
    "s_memrealtime s[78:79]\n"
    "v_smfmac_f32_16x16x64_bf16 v[60:63], v[36:39], a[64:71], v1\n"
    "ds_read_b128 v[40:43], v109 offset:53248\n"
    "v_smfmac_f32_16x16x64_bf16 v[68:71], v[32:35], a[56:63], v1\n"
    "ds_read_b128 v[44:47], v110 offset:53248\n"
    "v_smfmac_f32_16x16x64_bf16 v[72:75], v[32:35], a[184:191], v1\n"
    "ds_read_b128 v[48:51], v111 offset:53248\n"
    "v_smfmac_f32_16x16x64_bf16 v[68:71], v[36:39], a[72:79], v1\n"
    "ds_read_b128 v[52:55], v0 offset:57344\n"
    "v_smfmac_f32_16x16x64_bf16 v[72:75], v[36:39], a[192:199], v1\n"
    "ds_read_b128 v[56:59], v0 offset:58368\n"
    "s_waitcnt lgkmcnt(4)\n"
    "v_smfmac_f32_16x16x64_bf16 v[64:67], v[40:43], v[80:87], v1\n"
    "ds_read_b128 v[80:83], v8 offset:16384\n"
    "v_smfmac_f32_16x16x64_bf16 v[60:63], v[40:43], a[80:87], v1\n"
    "ds_read_b128 v[84:87], v8 offset:17408\n"
    "s_waitcnt lgkmcnt(5)\n"
    "v_smfmac_f32_16x16x64_bf16 v[64:67], v[44:47], v[88:95], v1\n"
    "ds_read_b128 v[88:91], v8 offset:18432\n"
    "v_smfmac_f32_16x16x64_bf16 v[60:63], v[44:47], a[96:103], v1\n"
    "ds_read_b128 v[92:95], v8 offset:19456\n"
    "s_waitcnt lgkmcnt(4)\n"
    "v_smfmac_f32_16x16x64_bf16 v[64:67], v[48:51], v[96:103], v1\n"
    "ds_read_b128 v[96:99], v15 offset:12288\n"
    "v_smfmac_f32_16x16x64_bf16 v[60:63], v[48:51], a[112:119], v1\n"
    "ds_read_b128 v[100:103], v15 offset:16384\n"
    "v_smfmac_f32_16x16x64_bf16 v[60:63], v[52:55], a[128:135], v1\n"
    "s_cmp_gt_u32 s78, s86\n"
    "v_smfmac_f32_16x16x64_bf16 v[60:63], v[56:59], a[144:151], v1\n"
    "s_cbranch_scc1 pub_%=\n"
    "pubok_%=:\n"
    "s_waitcnt lgkmcnt(4)\n"
    "v_smfmac_f32_16x16x64_bf16 v[64:67], v[52:55], v[80:87], v1\n"
    "s_waitcnt lgkmcnt(2)\n"
    "v_smfmac_f32_16x16x64_bf16 v[64:67], v[56:59], v[88:95], v1\n"
    "v_add_f32 v60, v60, v62\n"
    "v_smfmac_f32_16x16x64_bf16 v[68:71], v[40:43], a[88:95], v1\n"
    "v_add_f32 v61, v61, v63\n"
    "v_smfmac_f32_16x16x64_bf16 v[72:75], v[40:43], a[200:207], v1\n"
    "v_cvt_pk_bf16_f32 v60, v60, v61\n"
    "v_smfmac_f32_16x16x64_bf16 v[68:71], v[44:47], a[104:111], v1\n"
    "v_add_f32 v64, v64, v66\n"
    "v_smfmac_f32_16x16x64_bf16 v[72:75], v[44:47], a[208:215], v1\n"
    "v_add_f32 v65, v65, v67\n"
    "v_smfmac_f32_16x16x64_bf16 v[68:71], v[48:51], a[120:127], v1\n"
    "v_cvt_pk_bf16_f32 v61, v64, v65\n"
    "v_smfmac_f32_16x16x64_bf16 v[72:75], v[48:51], a[216:223], v1\n"
    "ds_write_b64 v2, v[60:61]\n"
    "v_smfmac_f32_16x16x64_bf16 v[68:71], v[52:55], a[136:143], v1\n"
    "buffer_store_dword v61, v6, s[60:63], s92 offen offset:0 sc1\n"
    "v_smfmac_f32_16x16x64_bf16 v[72:75], v[52:55], a[224:231], v1\n"
    "ds_read_b128 v[80:83], v15 offset:13312\n"
    "v_smfmac_f32_16x16x64_bf16 v[68:71], v[56:59], a[152:159], v1\n"
    "s_waitcnt lgkmcnt(1)\n"
    "s_barrier\n"
    "v_smfmac_f32_16x16x64_bf16 v[72:75], v[56:59], a[232:239], v1\n"
    "ds_read_b64_tr_b16 v[60:61], v3 offset:0\n"
    "ds_read_b64_tr_b16 v[62:63], v3 offset:128\n"
    "v_smfmac_f32_16x16x64_bf16 v[76:79], v[24:27], v[184:191], v1\n"
    "v_add_f32 v68, v68, v70\n"
    "v_add_f32 v69, v69, v71\n"
    "v_cvt_pk_bf16_f32 v68, v68, v69\n"
    "s_nop 0\n"
    "v_add_f32 v72, v72, v74\n"
    "v_add_f32 v73, v73, v75\n"
    "v_cvt_pk_bf16_f32 v69, v72, v73\n"
    "ds_write_b64 v2, v[68:69] offset:3072\n"
    "v_smfmac_f32_16x16x64_bf16 v[76:79], v[28:31], v[192:199], v1\n"
    "ds_read_b64_tr_b16 v[64:65], v3 offset:1536\n"
    "ds_read_b64_tr_b16 v[66:67], v3 offset:1664\n"
    "s_waitcnt lgkmcnt(3)\n"
    "v_mfma_f32_16x16x32_bf16 v[108:111], v[60:63], v[60:63], 0\n"
    "ds_read_b128 v[84:87], v15 offset:17408\n"
    "buffer_store_dword v69, v6, s[60:63], s92 offen offset:1024 sc1\n"
    "v_mfma_f32_16x16x32_bf16 v[20:23], v[96:99], v[60:63], 0\n"
    "s_waitcnt lgkmcnt(3)\n"
    "s_barrier\n"
    "v_mfma_f32_16x16x32_bf16 v[24:27], v[100:103], v[60:63], 0\n"
    "ds_read_b64_tr_b16 v[68:69], v3 offset:3072\n"
    "ds_read_b64_tr_b16 v[70:71], v3 offset:3200\n"
    "v_smfmac_f32_16x16x64_bf16 v[76:79], v[32:35], v[200:207], v1\n"
    "ds_read_b128 v[88:91], v15 offset:14336\n"
    "ds_read_b128 v[92:95], v15 offset:18432\n"
    "s_waitcnt lgkmcnt(5)\n"
    "v_mfma_f32_16x16x32_bf16 v[108:111], v[64:67], v[64:67], v[108:111]\n"
    "ds_read_b64_tr_b16 v[72:73], v3 offset:4608\n"
    "ds_read_b64_tr_b16 v[74:75], v3 offset:4736\n"
    "v_mfma_f32_16x16x32_bf16 v[20:23], v[80:83], v[64:67], v[20:23]\n"
    "ds_read_b128 v[28:31], v15 offset:15360\n"
    "ds_read_b128 v[32:35], v15 offset:19456\n"
    "s_waitcnt lgkmcnt(8)\n"
    "v_mfma_f32_16x16x32_bf16 v[24:27], v[84:87], v[64:67], v[24:27]\n"
    "v_smfmac_f32_16x16x64_bf16 v[76:79], v[36:39], v[208:215], v1\n"
    "s_waitcnt lgkmcnt(4)\n"
    "v_mfma_f32_16x16x32_bf16 v[108:111], v[68:71], v[68:71], v[108:111]\n"
    "buffer_load_dwordx4 v[96:99], v4, s[56:59], s87 offen sc1\n"
    "ds_read_b128 v[80:83], v15 offset:20480\n"
    "v_mfma_f32_16x16x32_bf16 v[20:23], v[88:91], v[68:71], v[20:23]\n"
    "buffer_load_dwordx4 v[100:103], v4, s[56:59], s40 offen sc1\n"
    "ds_read_b128 v[84:87], v15 offset:21504\n"
    "v_mfma_f32_16x16x32_bf16 v[24:27], v[92:95], v[68:71], v[24:27]\n"
    "buffer_load_dwordx4 v[104:107], v4, s[56:59], s41 offen sc1\n"
    "s_waitcnt lgkmcnt(2)\n"
    "v_mfma_f32_16x16x32_bf16 v[108:111], v[72:75], v[72:75], v[108:111]\n"
    "s_mov_b64 vcc, s[88:89]\n"
    "ds_read_b128 v[88:91], v15 offset:22528\n"
    "v_mfma_f32_16x16x32_bf16 v[20:23], v[28:31], v[72:75], v[20:23]\n"
    "v_mov_b64 v[60:61], 0\n"
    "ds_read_b128 v[92:95], v15 offset:23552\n"
    "v_mfma_f32_16x16x32_bf16 v[24:27], v[32:35], v[72:75], v[24:27]\n"
    "v_mov_b64 v[62:63], 0\n"
    "v_smfmac_f32_16x16x64_bf16 v[76:79], v[40:43], v[216:223], v1\n"
    "v_max3_f32 v64, v110, v111, v20\n"
    "v_max3_f32 v64, v64, v21, v22\n"
    "v_smfmac_f32_16x16x64_bf16 v[76:79], v[44:47], v[224:231], v1\n"
    "v_max3_f32 v64, v64, v23, v24\n"
    "v_max3_f32 v64, v64, v25, v26\n"
    "v_smfmac_f32_16x16x64_bf16 v[76:79], v[48:51], v[232:239], v1\n"
    "v_max_f32 v65, v64, v27\n"
    "v_max_f32 v64, v64, v27\n"
    "v_smfmac_f32_16x16x64_bf16 v[76:79], v[52:55], v[240:247], v1\n"
    "v_cndmask_b32_dpp v68, v24, v20, vcc quad_perm:[0,1,0,1] row_mask:0xf bank_mask:0xf\n"
    "v_permlane32_swap_b32 v64, v65\n"
    "v_max_f32 v66, v64, v65\n"
    "v_max_f32 v64, v64, v65\n"
    "v_smfmac_f32_16x16x64_bf16 v[76:79], v[56:59], v[248:255], v1\n"
    "v_cndmask_b32_dpp v69, v25, v21, vcc quad_perm:[0,1,0,1] row_mask:0xf bank_mask:0xf\n"
    "v_permlane16_swap_b32 v64, v66\n"
    "v_max_f32 v64, v64, v66\n"
    "v_cndmask_b32_dpp v70, v26, v22, vcc quad_perm:[0,1,0,1] row_mask:0xf bank_mask:0xf\n"
    "v_cndmask_b32_dpp v71, v27, v23, vcc quad_perm:[0,1,0,1] row_mask:0xf bank_mask:0xf\n"
    "v_mul_f32_dpp v66, v64, v14 quad_perm:[0,1,0,1] row_mask:0xf bank_mask:0xf\n"
    "v_pk_fma_f32 v[68:69], v[68:69], s[94:95], v[66:67] op_sel_hi:[1,1,0]\n"
    "v_pk_fma_f32 v[70:71], v[70:71], s[94:95], v[66:67] op_sel_hi:[1,1,0]\n"
    "v_add_f32 v76, v76, v78\n"
    "v_add_f32 v77, v77, v79\n"
    "v_cvt_pk_bf16_f32 v76, v76, v77\n"
    "ds_write_b32 v6, v76\n"
    "v_exp_f32 v68, v68\n"
    "v_exp_f32 v69, v69\n"
    "v_exp_f32 v70, v70\n"
    "v_exp_f32 v71, v71\n"
    "v_mov_b32_dpp v108, v110 quad_perm:[0,1,0,1] row_mask:0xf bank_mask:0xf\n"
    "v_cvt_pk_bf16_f32 v52, v68, v69\n"
    "v_cvt_pk_bf16_f32 v53, v70, v71\n"
    "v_mov_b32_dpp v109, v111 quad_perm:[0,1,0,1] row_mask:0xf bank_mask:0xf\n"
    "s_waitcnt lgkmcnt(0)\n"
    "s_barrier\n"
    "v_mov_b32_dpp v54, v52 quad_perm:[2,3,0,1] row_mask:0xf bank_mask:0xf\n"
    "v_mov_b32_dpp v55, v53 quad_perm:[2,3,0,1] row_mask:0xf bank_mask:0xf\n"
    "ds_read2_b32 v[56:57], v7 offset0:0 offset1:128\n"
    "ds_read2_b32 v[58:59], v7 offset0:64 offset1:192\n"
    "v_mfma_f32_16x16x32_bf16 v[72:75], v[80:83], v[52:55], 0\n"
    "v_pk_fma_f32 v[108:109], v[108:109], s[94:95], v[66:67] op_sel_hi:[1,1,0]\n"
    "v_mfma_f32_16x16x32_bf16 v[20:23], v[88:91], v[52:55], 0\n"
    "v_exp_f32 v108, v108\n"
    "v_exp_f32 v109, v109\n"
    "v_mfma_f32_16x16x32_bf16 v[48:51], v[16:19], v[52:55], 0\n"
    "buffer_store_dword v76, v6, s[60:63], s92 offen offset:2048 sc1\n"
    "v_cvt_pk_bf16_f32 v108, v108, v109\n"
    "v_mfma_f32_16x16x32_bf16 v[64:67], v[84:87], v[52:55], 0\n"
    "v_cndmask_b32 v110, 0, v108, vcc\n"
    "v_cndmask_b32 v111, v108, 0, vcc\n"
    "v_mfma_f32_16x16x32_bf16 v[24:27], v[92:95], v[52:55], 0\n"
    "v_cndmask_b32 v72, v20, v72, vcc\n"
    "v_cndmask_b32 v73, v21, v73, vcc\n"
    "v_mfma_f32_16x16x16_bf16 v[48:51], v[16:17], v[110:111], v[48:51]\n"
    "v_cndmask_b32 v74, v22, v74, vcc\n"
    "v_cndmask_b32 v75, v23, v75, vcc\n"
    "s_cmp_eq_u32 s76, 63\n"
    "s_cbranch_scc1 done_%=\n"
    "s_waitcnt lgkmcnt(1)\n"
    "v_mfma_f32_16x16x16_bf16 v[72:75], v[56:57], v[110:111], v[72:75]\n"
    "v_cndmask_b32 v64, v24, v64, vcc\n"
    "v_cndmask_b32 v65, v25, v65, vcc\n"
    "v_cndmask_b32 v66, v26, v66, vcc\n"
    "v_cndmask_b32 v67, v27, v67, vcc\n"
    "v_rcp_f32 v108, v48\n"
    "v_mov_b64 v[76:77], 0\n"
    "s_waitcnt lgkmcnt(0)\n"
    "v_mfma_f32_16x16x16_bf16 v[64:67], v[58:59], v[110:111], v[64:67]\n"
    "v_mfma_f32_32x32x16_bf16 v[28:43], v[60:63], v[60:63], 0\n"
    "v_mul_f32 v72, v72, v108\n"
    "v_mul_f32 v73, v73, v108\n"
    "v_mul_f32 v74, v74, v108\n"
    "v_mul_f32 v75, v75, v108\n"
    "v_cvt_pk_bf16_f32 v52, v72, v73\n"
    "v_cvt_pk_bf16_f32 v53, v74, v75\n"
    "v_pk_mul_f32 v[64:65], v[64:65], v[108:109] op_sel_hi:[1,0]\n"
    "v_pk_mul_f32 v[66:67], v[66:67], v[108:109] op_sel_hi:[1,0]\n"
    "v_cvt_pk_bf16_f32 v54, v64, v65\n"
    "v_cvt_pk_bf16_f32 v55, v66, v67\n"
    "v_mov_b64 v[78:79], 0\n"
    "v_mov_b32_dpp v44, v52 quad_perm:[2,3,0,1] row_mask:0xf bank_mask:0xf\n"
    "v_smfmac_f32_16x16x64_bf16 v[28:31], v[52:55], a[240:247], v1\n"
    "v_mov_b32_dpp v45, v53 quad_perm:[2,3,0,1] row_mask:0xf bank_mask:0xf\n"
    "v_smfmac_f32_16x16x64_bf16 v[32:35], v[52:55], v[112:119], v1\n"
    "v_mov_b32_dpp v46, v54 quad_perm:[2,3,0,1] row_mask:0xf bank_mask:0xf\n"
    "v_mov_b32_dpp v47, v55 quad_perm:[2,3,0,1] row_mask:0xf bank_mask:0xf\n"
    "v_smfmac_f32_16x16x64_bf16 v[36:39], v[52:55], v[128:135], v1\n"
    "v_smfmac_f32_16x16x64_bf16 v[28:31], v[44:47], a[248:255], v1\n"
    "v_smfmac_f32_16x16x64_bf16 v[32:35], v[44:47], v[120:127], v1\n"
    "v_smfmac_f32_16x16x64_bf16 v[40:43], v[52:55], v[144:151], v1\n"
    "v_smfmac_f32_16x16x64_bf16 v[36:39], v[44:47], v[136:143], v1\n"
    "v_cvt_pk_bf16_f32 v60, v28, v29\n"
    "v_cvt_pk_bf16_f32 v61, v30, v31\n"
    "v_cvt_pk_bf16_f32 v62, v32, v33\n"
    "v_cvt_pk_bf16_f32 v63, v34, v35\n"
    "v_smfmac_f32_16x16x64_bf16 v[40:43], v[44:47], v[152:159], v1\n"
    "buffer_store_dwordx4 v[60:63], v9, s[52:55], s77 offen sc1\n"
    "v_smfmac_f32_16x16x64_bf16 v[76:79], v[52:55], v[160:167], v1\n"
    "v_cvt_pk_bf16_f32 v64, v36, v37\n"
    "v_smfmac_f32_16x16x64_bf16 v[76:79], v[44:47], v[168:175], v1\n"
    "v_cvt_pk_bf16_f32 v65, v38, v39\n"
    "v_cvt_pk_bf16_f32 v66, v40, v41\n"
    "v_cvt_pk_bf16_f32 v67, v42, v43\n"
    "buffer_store_dwordx4 v[64:67], v10, s[52:55], s77 offen sc1\n"
    "s_nop 3\n"
    "v_cvt_pk_bf16_f32 v68, v76, v77\n"
    "v_cvt_pk_bf16_f32 v69, v78, v79\n"
    "buffer_store_dwordx2 v[68:69], v11, s[52:55], s77 offen sc1\n"
    "s_memrealtime s[48:49]\n"
    "s_mov_b64 exec, 6\n"
    "buffer_load_dword v108, v0, s[64:67], s69 offen sc1\n"
    "s_mov_b32 exec_lo, s71\n"
    "v_lshlrev_b32 v109, 4, v0\n"
    "buffer_load_dword v20, v109, s[52:55], s38 offen offset:2048 sc1\n"
    "s_mov_b64 exec, -1\n"
    "s_add_u32 s76, s76, 1\n"
    "s_xor_b32 s77, s77, 0x202000\n"
    "s_add_u32 s36, s36, 32768\n"
    "s_add_u32 s37, s37, 32768\n"
    "s_add_u32 s38, s38, 32768\n"
    "s_bitcmp1_b32 s76, 0\n"
    "s_cselect_b32 s92, 8192, 0\n"
    "s_cselect_b32 s81, 1, 2\n"
    "s_lshl_b32 s81, s81, 21\n"
    "s_add_u32 s91, s91, s81\n"
    "s_xor_b32 s91, s91, s92\n"
    "s_sub_u32 s92, s91, 8192\n"
    "s_waitcnt lgkmcnt(0)\n"
    "s_lshl_b32 s100, s48, 18\n"
    "s_lshr_b32 s81, s101, 3\n"
    "s_sub_u32 s101, s101, s81\n"
    "s_add_u32 s81, s81, 8\n"
    "s_sub_u32 s98, s48, s78\n"
    "s_min_u32 s98, s98, s81\n"
    "s_add_u32 s101, s101, s98\n"
    "s_lshr_b32 s98, s101, 3\n"
    "s_add_u32 s98, s98, s72\n"
    "ds_read_b128 v[80:83], v8 offset:0\n"
    "ds_read_b128 v[84:87], v8 offset:1024\n"
    "ds_read_b128 v[88:91], v8 offset:2048\n"
    "ds_read_b128 v[92:95], v8 offset:3072\n"
    "ds_read_b128 v[44:47], v8 offset:6144\n"
    "ds_read_b128 v[48:51], v8 offset:7168\n"
    "ds_read_b128 v[52:55], v8 offset:8192\n"
    "ds_read_b128 v[56:59], v8 offset:9216\n"
    "v_mov_b64 v[60:61], 0\n"
    "v_mov_b64 v[62:63], 0\n"
    "v_mov_b64 v[64:65], 0\n"
    "v_mov_b64 v[66:67], 0\n"
    "v_mov_b64 v[68:69], 0\n"
    "v_mov_b64 v[70:71], 0\n"
    "v_mov_b64 v[72:73], 0\n"
    "v_mov_b64 v[74:75], 0\n"
    "v_mov_b64 v[76:77], 0\n"
    "v_mov_b64 v[78:79], 0\n"
    "v_xor_b32 v15, 12288, v15\n"
    "s_waitcnt vmcnt(0)\n"
    "v_readfirstlane_b32 s85, v20\n"
    "v_cmp_ne_u32 vcc, -1, v20\n"
    "v_readlane_b32 s93, v108, 1\n"
    "v_readlane_b32 s82, v108, 2\n"
    "s_and_b32 s84, s85, 0xfffc0000\n"
    "s_and_b32 s81, s85, 0x1ff\n"
    "s_cmp_lg_u32 s85, -1\n"
    "s_cselect_b32 s46, s84, s46\n"
    "s_cselect_b32 s75, s81, s75\n"
    "s_mov_b32 s35, 0\n"
    "s_and_b32 s81, vcc_lo, 30\n"
    "s_and_b32 s81, s81, s71\n"
    "s_cbranch_scc1 sh_%=\n"
    "shok_%=:\n"
    "s_sub_u32 s100, s100, s46\n"
    "s_add_u32 s46, s46, s35\n"
    "s_ashr_i32 s100, s100, 18\n"
    "s_sub_u32 s86, s48, s100\n"
    "s_sub_u32 s86, s86, s98\n"
    "s_max_i32 s100, s100, 1\n"
    "s_add_u32 s81, s75, s74\n"
    "s_add_u32 s86, s86, s81\n"
    "s_add_u32 s86, s86, s75\n"
    "s_add_u32 s86, s86, s73\n"
    "s_lshr_b32 s35, s35, 18\n"
    "s_add_u32 s86, s86, s35\n"
    "s_cmp_lg_u32 s70, 0\n"
    "s_cselect_b32 s86, s86, -1\n"
    "s_lshl_b32 s81, s81, 18\n"
    "s_add_u32 s46, s46, s81\n"
    "s_min_u32 s93, s93, s82\n"
    "s_cmp_lg_u32 s42, 0\n"
    "s_cbranch_scc1 late_%=\n"
    "lateok_%=:\n"
    "v_or_b32 v108, s99, v4\n"
    "ds_write_b128 v108, v[96:99] offset:12288\n"
    "ds_write_b128 v108, v[100:103] offset:16384\n"
    "ds_write_b128 v108, v[104:107] offset:20480\n"
    "s_xor_b32 s99, s99, 12288\n"
    "ds_read_b128 v[96:99], v8 offset:4096\n"
    "ds_read_b128 v[100:103], v8 offset:5120\n"
    "s_add_u32 s31, s76, 1\n"
    "s_min_u32 s31, s31, 63\n"
    "s_cmp_lt_u32 s31, s93\n"
    "s_cselect_b32 s42, 0, 1\n"
    "s_lshr_b32 s81, s31, 1\n"
    "s_add_u32 s81, s31, s81\n"
    "s_lshl_b32 s87, s81, 21\n"
    "s_xor_b32 s81, s87, s91\n"
    "s_lshr_b32 s81, s81, 8\n"
    "s_xor_b32 s81, s81, s91\n"
    "s_and_b32 s81, s81, 8192\n"
    "s_add_u32 s87, s87, s81\n"
    "s_add_u32 s40, s87, 4096\n"
    "s_add_u32 s41, s87, s34\n"
    "v_xor_b32 v109, 1024, v4\n"
    "v_xor_b32 v110, 2048, v4\n"
    "v_xor_b32 v111, 3072, v4\n"
    "v_mov_b32 v104, s76\n"
    "v_mov_b32 v105, s100\n"
    "s_cmp_eq_u32 s70, 0\n"
    "s_cbranch_scc1 gob_%=\n"
    "s_sub_u32 s84, s46, 0x20000000\n"
    "utb_%=:\n"
    "s_memrealtime s[48:49]\n"
    "s_waitcnt lgkmcnt(0)\n"
    "s_lshl_b32 s81, s48, 18\n"
    "s_sub_u32 s81, s81, s84\n"
    "s_cmp_lt_u32 s81, 0x1ff40000\n"
    "s_cbranch_scc1 utb_%=\n"
    "s_cmp_ge_u32 s81, 0x1ff80000\n"
    "s_cbranch_scc1 utbf2_%=\n"
    "s_nop 7\n"
    "utbf2_%=:\n"
    "s_cmp_ge_u32 s81, 0x1ffc0000\n"
    "s_cbranch_scc1 utbf1_%=\n"
    "s_nop 7\n"
    "utbf1_%=:\n"
    "s_cmp_ge_u32 s81, 0x20000000\n"
    "s_cbranch_scc1 utbf0_%=\n"
    "s_nop 7\n"
    "utbf0_%=:\n"
    "gob_%=:\n"
    "s_waitcnt lgkmcnt(0)\n"
    "s_barrier\n"
    "s_branch loop_%=\n"
    "late_%=:\n"
    "s_cmp_lt_u32 s31, s93\n"
    "s_cbranch_scc1 kvokl_%=\n"
    "s_mov_b32 s90, 0\n"
    "kvpl_%=:\n"
    "buffer_load_dword v108, off, s[64:67], s69 offset:16 sc1\n"
    "buffer_load_dword v109, off, s[64:67], s69 offset:32 sc1\n"
    "s_waitcnt vmcnt(0)\n"
    "v_min_u32 v108, v108, v109\n"
    "s_nop 0\n"
    "v_readfirstlane_b32 s93, v108\n"
    "s_cmp_lt_u32 s31, s93\n"
    "s_cbranch_scc1 kvokl_%=\n"
    "s_sleep 2\n"
    "s_add_u32 s90, s90, 1\n"
    "s_or_b32 s81, s90, s80\n"
    "s_cmp_lt_u32 s81, 262144\n"
    "s_cbranch_scc1 kvpl_%=\n"
    "s_mov_b32 s80, 262144\n"
    "kvokl_%=:\n"
    "buffer_load_dwordx4 v[96:99], v4, s[56:59], s87 offen sc1\n"
    "buffer_load_dwordx4 v[100:103], v4, s[56:59], s40 offen sc1\n"
    "buffer_load_dwordx4 v[104:107], v4, s[56:59], s41 offen sc1\n"
    "s_waitcnt vmcnt(0)\n"
    "s_branch lateok_%=\n"
    "sh_%=:\n"
    "v_readlane_b32 s81, v20, 1\n"
    "v_readlane_b32 s78, v20, 2\n"
    "v_readlane_b32 s79, v20, 3\n"
    "v_readlane_b32 s86, v20, 4\n"
    "s_min_u32 s81, s81, s78\n"
    "s_min_u32 s81, s81, s79\n"
    "s_min_u32 s81, s81, s86\n"
    "s_not_b32 s81, s81\n"
    "s_sub_u32 s81, s81, s75\n"
    "s_lshl_b32 s81, s81, 18\n"
    "s_sub_u32 s81, s81, s46\n"
    "s_cmp_lt_u32 s81, 0x20000000\n"
    "s_cselect_b32 s81, s81, 0\n"
    "s_cmp_ge_u32 s81, s43\n"
    "s_cselect_b32 s35, s81, 0\n"
    "s_branch shok_%=\n"
    "pub_%=:\n"
    "s_sub_u32 s81, s78, s86\n"
    "s_cmp_gt_u32 s81, 20\n"
    "s_cselect_b32 s81, 6, 0\n"
    "s_add_u32 s98, s98, s81\n"
    "s_cmp_gt_u32 s81, s39\n"
    "s_cbranch_scc1 pubok_%=\n"
    "s_add_u32 s81, s78, s98\n"
    "s_not_b32 s81, s81\n"
    "v_mov_b32 v5, s81\n"
    "s_add_u32 s81, s38, s68\n"
    "s_mov_b64 exec, 1\n"
    "buffer_atomic_umin v5, off, s[52:55], s81 offset:2304 sc1\n"
    "s_mov_b64 exec, -1\n"
    "s_branch pubok_%=\n"
    "fail0_%=:\n"
    "s_mov_b32 s90, 0\n"
    "s_waitcnt vmcnt(0) lgkmcnt(0)\n"
    "fl0_%=:\n"
    "s_nop 7\n"
    "buffer_load_dwordx4 v[20:23], v4, s[52:55], s36 offen sc1\n"
    "buffer_load_dwordx4 v[36:39], v4, s[52:55], s37 offen sc1\n"
    "buffer_load_dwordx2 v[40:41], v13, s[52:55], s38 offen sc1\n"
    "v_mov_b64 v[60:61], 0\n"
    "v_mov_b64 v[62:63], 0\n"
    "v_mov_b64 v[64:65], 0\n"
    "v_mov_b64 v[66:67], 0\n"
    "s_memrealtime s[48:49]\n"
    "s_waitcnt lgkmcnt(0)\n"
    "s_add_u32 s81, s48, s98\n"
    "s_add_u32 s81, s81, 60\n"
    "s_not_b32 s81, s81\n"
    "v_mov_b32 v5, s81\n"
    "s_add_u32 s81, s38, s68\n"
    "s_mov_b64 exec, 1\n"
    "s_cmp_lg_u32 s39, 0\n"
    "s_cbranch_scc1 pb0a_%=\n"
    "v_mov_b32 v108, 0x7fffffff\n"
    "buffer_store_dword v5, v108, s[52:55], 0 offen sc1\n"
    "s_branch pb0b_%=\n"
    "pb0a_%=:\n"
    "buffer_atomic_umin v5, off, s[52:55], s81 offset:2304 sc1\n"
    "pb0b_%=:\n"
    "s_mov_b64 exec, -1\n"
    "s_waitcnt vmcnt(3)\n"
    "v_max3_u32 v108, v20, v21, v22\n"
    "v_max_u32 v108, v108, v23\n"
    "v_cmp_eq_u32 vcc, -1, v108\n"
    "s_waitcnt vmcnt(1) lgkmcnt(0)\n"
    "s_nop 4\n"
    "s_cbranch_vccz ret0_%=\n"
    "s_add_u32 s90, s90, 1\n"
    "s_or_b32 s81, s90, s80\n"
    "s_cmp_lt_u32 s81, 262144\n"
    "s_cbranch_scc1 fl0_%=\n"
    "s_mov_b32 s80, 262144\n"
    "v_mov_b32 v20, 0\n"
    "v_mov_b32 v21, 0\n"
    "v_mov_b32 v22, 0\n"
    "v_mov_b32 v23, 0\n"
    "s_nop 1\n"
    "s_branch ret0_%=\n"
    "fail4_%=:\n"
    "s_mov_b32 s90, 0\n"
    "s_waitcnt vmcnt(0) lgkmcnt(0)\n"
    "fl4_%=:\n"
    "s_nop 7\n"
    "buffer_load_dwordx4 v[36:39], v4, s[52:55], s37 offen sc1\n"
    "buffer_load_dwordx2 v[40:41], v13, s[52:55], s38 offen sc1\n"
    "s_memrealtime s[48:49]\n"
    "s_waitcnt lgkmcnt(0)\n"
    "s_add_u32 s81, s48, s98\n"
    "s_add_u32 s81, s81, 40\n"
    "s_not_b32 s81, s81\n"
    "v_mov_b32 v5, s81\n"
    "s_add_u32 s81, s38, s68\n"
    "s_mov_b64 exec, 1\n"
    "s_cmp_lg_u32 s39, 0\n"
    "s_cbranch_scc1 pb4a_%=\n"
    "v_mov_b32 v108, 0x7fffffff\n"
    "buffer_store_dword v5, v108, s[52:55], 0 offen sc1\n"
    "s_branch pb4b_%=\n"
    "pb4a_%=:\n"
    "buffer_atomic_umin v5, off, s[52:55], s81 offset:2304 sc1\n"
    "pb4b_%=:\n"
    "s_mov_b64 exec, -1\n"
    "s_waitcnt vmcnt(1)\n"
    "v_max3_u32 v108, v36, v37, v38\n"
    "v_max3_u32 v108, v108, v39, v40\n"
    "v_max_u32 v108, v108, v41\n"
    "ds_write_b128 v4, v[36:39] offset:53248\n"
    "ds_write_b64 v13, v[40:41] offset:57344\n"
    "v_cmp_eq_u32 vcc, -1, v108\n"
    "s_waitcnt vmcnt(1) lgkmcnt(0)\n"
    "s_nop 4\n"
    "s_cbranch_vccz ret4_%=\n"
    "s_add_u32 s90, s90, 1\n"
    "s_or_b32 s81, s90, s80\n"
    "s_cmp_lt_u32 s81, 262144\n"
    "s_cbranch_scc1 fl4_%=\n"
    "s_mov_b32 s80, 262144\n"
    "s_branch ret4_%=\n"
    "done_%=:\n"
    "s_memrealtime s[82:83]\n"
    "s_waitcnt vmcnt(0) lgkmcnt(0)\n"
    "s_barrier\n"
    "v_mov_b32 v96, 64\n"
    "buffer_store_dword v96, v12, s[64:67], 0 offen sc1\n"
    "s_waitcnt vmcnt(0)\n"
        ".Lk1:\n"
        : "+{v[0:15]}"(cv), "+{v[20:35]}"(pv0), "+{v[36:51]}"(pv1), "+{s[36:51]}"(sa), "+{s[52:67]}"(sb), "+{s[68:75]}"(sc)
        :
        : "memory", "vcc", "scc", "m0", "v16", "v17", "v18", "v19", "v52", "v53", "v54", "v55", "v56", "v57", "v58", "v59", CL_60_255("v"),
          CL10("a", ), CL10("a", 1), CL10("a", 2), CL10("a", 3), CL10("a", 4), CL10("a", 5), CL_60_255("a"),
          "s30", "s31", "s34", "s35", "s76", "s77", "s78", "s79", CL10("s", 8), CL10("s", 9), "s100", "s101");
}

struct Ln { int tid, lane, w, g, c16, q4, p4; unsigned bud; };
DEVI Ln mkln() {
    Ln l; l.tid = threadIdx.x; l.lane = l.tid & 63; l.w = __builtin_amdgcn_readfirstlane(l.tid >> 6);
    l.g = l.lane >> 4; l.c16 = l.lane & 15; l.q4 = (l.lane >> 2) & 3; l.p4 = l.lane & 3; l.bud = SPIN; return l;
}
DEVI int swz1(int row) { return ((row & 3) | (((row >> 3) & 1) << 2)) << 1; }
DEVI void load_slab1(const Ln& l, const uchar* a, const uchar* b, unsigned stride, const int (&un)[2], const int (&by)[2], u32x4 (&wt)[3][20], const uchar* s3) {
    {
        const int row = 4 * l.w + l.g, u = l.c16 ^ swz1(row);
        const uchar* src = (u < 8 ? a + u * 16 : b + (u - 8) * 16) + (size_t)row * stride;
#pragma unroll
        for (int i = 0; i < 40; ++i) glds16(src + (size_t)i * 16 * stride, i * 4096 + l.w * 1024);
    }
    unsigned ad[2][2];
#pragma unroll
    for (int i = 0; i < 2; ++i)
#pragma unroll
        for (int hh = 0; hh < 2; ++hh) { const int row = 8 * l.g + 4 * hh + l.q4; ad[i][hh] = row * 256 + ((un[i] ^ swz1(row)) * 16) + by[i]; }
#pragma unroll
    for (int ks = 0; ks < 20; ++ks) {
        if (ks == 0) VM_BAR(30);
        if (ks == 5 || ks == 10 || ks == 15) VM_BAR(20);
        if (ks == 5 || ks == 10) {
#pragma unroll
            for (int i = 2 * ks - 10; i < 2 * ks; ++i) glds16(s3 + (size_t)i * 32 * stride, i * 4096 + l.w * 1024);
        }
#pragma unroll
        for (int i = 0; i < 2; ++i) wt[i][ks] = trd2(ad[i][0], ks * 8192, ad[i][1], ks * 8192);
    }
}
template <int NK, int NT> DEVI void load_slab2(const Ln& l, const uchar* a, u32x4 (&wt)[5][2][NT], const int k0) {
#pragma unroll
    for (int i = 0; i < 10 * NK; ++i) {
        const int n = i * 256 + l.tid, row = n / 80, pos = n - row * 80;
        glds16(a + (row * 80 + (pos ^ ((row & 3) << 2))) * 16, i * 4096 + l.w * 1024);
    }
    VM_BAR(0);
#pragma unroll
    for (int P = 0; P < 5; ++P) {
        const unsigned ad = (4 * l.g + l.q4) * 1280 + ((((P < 4 ? 16 * l.w + 4 * P : 64 + 4 * l.w) + l.p4) ^ (l.q4 << 2)) * 16);
#pragma unroll
        for (int i = 0; i < 2; ++i)
#pragma unroll
            for (int k = 0; k < NK; ++k) wt[P][i][k0 + k] = trd2(ad, 8 * i + k * 40960, ad, 8 * i + k * 40960 + 20480);
    }
    LDS_BAR();
}
DEVI void act_load(rsrc_t r, const Ln& l, unsigned soff, u32x4 (&t)[10]) {
#pragma unroll
    for (int i = 0; i < 10; ++i) t[i] = ld128(r, l.tid * 16, soff + i * 4096);
}
#define SB() __builtin_amdgcn_sched_barrier(0)
template <int I, int N, class F> DEVI void sfor(F&& f) { if constexpr (I < N) { f(std::integral_constant<int, I>{}); sfor<I + 1, N>(f); } }
template <bool SW, bool AG, bool Z> DEVI void mfa(f32x4& c, const u32x4& w, const u32x4& f) {
#define MFA(a, b, k) do { if (Z) asm volatile("v_mfma_f32_16x16x32_bf16 %0, " a ", " b ", 0" : "=&v"(c) : k(w), "v"(f)); \
                          else asm volatile("v_mfma_f32_16x16x32_bf16 %0, " a ", " b ", %0" : "+v"(c) : k(w), "v"(f)); } while (0)
    if (SW) { if (AG) MFA("%2", "%1", "a"); else MFA("%2", "%1", "v"); } else { if (AG) MFA("%1", "%2", "a"); else MFA("%1", "%2", "v"); }
}
template <int N> DEVI void pin_a(u32x4 (&a)[N]) {
#pragma unroll
    for (int i = 0; i < N; ++i) asm volatile("" : "+a"(a[i]));
}
struct Pg {
    unsigned cs, hf, hw;
    DEVI unsigned page(unsigned pg, unsigned fl) const { return W_END + P_SEG + pg * PAGE + ((pg ^ cs ^ fl) & 1) * 8192; }
    DEVI unsigned kv(int s) const { return page(s + (s >> 1), 1); }
    DEVI unsigned un(int s) const { s &= 64 - 1; return page(s + (s >> 1), 0); }
    DEVI unsigned fp(int s) const { s &= 64 - 1; return page(3 * (s >> 1) + 2, 0) + ((s & 1) * 12 - 6) * UN; }
};
DEVI Pg mkpg(const Prm& p, unsigned f) { const unsigned c = (__builtin_amdgcn_readfirstlane(f) & 255) > 16; return Pg{c, (c ^ 1 ^ ((p.flip >> 4) & 1)) * 8192, (c ^ ((p.flip >> 5) & 1)) * 8192}; }
template <int N> DEVI void wait_mod(rsrc_t r, Ln& l, const unsigned (&voff)[N], u32x4 (&v)[N]) {
#pragma unroll 1
    for (;;) {
        asm volatile("" ::: "memory");
        unsigned m = 1;
#pragma unroll
        for (int i = 0; i < N; ++i) { v[i] = ld128(r, voff[i], W_MOD); m &= v[i][0] & v[i][1] & v[i][2] & v[i][3]; }
        if (__builtin_amdgcn_ballot_w64(m == 0) == 0 || l.bud == 0) break;
        --l.bud;
        __builtin_amdgcn_s_sleep(4);
    }
#pragma unroll
    for (int i = 0; i < N; ++i) v[i] &= 0xffff0000u;
}
DEVI void wg_init(const Ln& l, unsigned lw) { if (l.tid == 0) *LP(volatile unsigned, lw, 0) = 0; LDS_BAR(); }
DEVI void wait_wg(rsrc_t r, Ln& l, bool known, unsigned voff, unsigned val, unsigned lw, unsigned tag, int slow = 0) {
    if (l.w == 0) {
        if (!known) wait_ge(r, l.bud, voff, val, slow);
        *LP(volatile unsigned, lw, 0) = tag;
    } else if (!known) {
#pragma unroll 1
        while (__builtin_amdgcn_readfirstlane(*LP(volatile unsigned, lw, 0)) < tag && l.bud != 0) { --l.bud; __builtin_amdgcn_s_sleep(1); }
        asm volatile("" ::: "memory");
    }
}
DEVI void wait2(rsrc_t r, Ln& l, unsigned& known, unsigned voff, unsigned val) {
#pragma unroll 1
    while (known < val) {
        asm volatile("" ::: "memory");
        const unsigned f = ld32(r, voff, 0);
        known = min(min(__builtin_amdgcn_readlane(f, 0), __builtin_amdgcn_readlane(f, 1)), __builtin_amdgcn_readlane(f, 2));
        if (known >= val || l.bud == 0) break;
        --l.bud;
        __builtin_amdgcn_s_sleep(2);
    }
    asm volatile("" ::: "memory");
}

DEVI float silu_b(float c) { return rbf(c * __builtin_amdgcn_rcpf(1.0f + __builtin_amdgcn_exp2f(-1.4426950408889634f * c))); }
DEVI void mod_load(const Prm& p, const Ln& l, const int blk, u32x4 (&wm)[10], unsigned (&cc)[10]) {
#pragma unroll
    for (int k = 0; k < 10; ++k) {
        const int d = (l.tid >> 2) + 64 * k;
        wm[k] = *(const GAS u32x4*)(p.wmod + (size_t)d * 7680 + blk * 64 + (l.tid & 3) * 16);
        cc[k] = *(const GAS unsigned*)(p.c + (d >> 1) * 4);
    }
}
DEVI void mod_block(rsrc_t r, const Ln& l, const int blk, const u32x4 (&wm)[10], const unsigned (&cc)[10], const Cd& cd) {
    float acc[8];
#pragma unroll
    for (int e = 0; e < 8; ++e) acc[e] = 0;
#pragma unroll
    for (int k = 0; k < 10; ++k) {
        const float sv = silu_b((l.tid & 4) ? bhi(cc[k]) : blo(cc[k]));
#pragma unroll
        for (int e = 0; e < 4; ++e) { acc[2 * e] += sv * blo(wm[k][e]); acc[2 * e + 1] += sv * bhi(wm[k][e]); }
    }
#pragma unroll
    for (int e = 0; e < 8; ++e) {
        float v = acc[e]; v += dpp<0x124>(v); v += dpp<0x128>(v); v = xsum4(v);
        if (l.lane < 4) ldswf(l.w * 128 + l.lane * 32, e * 4, v);
    }
    gate(r, cd);
    LDS_BAR();
    if (l.tid < 32) {
        float v = rbf((ldsrf(l.tid * 4, 0) + ldsrf(l.tid * 4, 128)) + (ldsrf(l.tid * 4, 256) + ldsrf(l.tid * 4, 384)));
        if (blk >= 20) v = rbf(1.0f + v);
        st32(r, l.tid * 4, W_MOD + blk * 128, __float_as_uint(v) | 1u);
    }
    LDS_BAR();
}
DEVI void mod_rest(const Prm& p, rsrc_t r, const int x, const int j) {
    const Ln l = mkln();
    const int ch = min(27 * j + (l.tid & 31), 79), rg = l.tid >> 5;
    const uchar* src = p.wmod + rg * 7680 + (2 + x) * 1280 + ch * 16;
    u32x4 wm[2][20];
    auto load = [&](int b) {
#pragma unroll
        for (int k = 0; k < 20; ++k) wm[b & 1][k] = *(const GAS u32x4*)(src + (size_t)(20 * b + k) * 61440);
    };
    load(0);
#pragma unroll
    for (int k = 0; k < 2; ++k) {
        const int d = min(l.tid + 256 * k, 319);
        const unsigned c = *(const GAS unsigned*)(p.c + d * 4);
        ldsw64(d * 8, 0, u32x2{__float_as_uint(silu_b(blo(c))), __float_as_uint(silu_b(bhi(c)))});
    }
    LDS_BAR();
    float acc[8] = {0, 0, 0, 0, 0, 0, 0, 0};
#pragma unroll
    for (int b = 0; b < 4; ++b) {
        if (b < 3) load(b + 1);
#pragma unroll
        for (int k = 0; k < 20; ++k) {
            const float sv = ldsrf(rg * 4, (20 * b + k) * 32);
#pragma unroll
            for (int e = 0; e < 4; ++e) { acc[2 * e] += sv * blo(wm[b & 1][k][e]); acc[2 * e + 1] += sv * bhi(wm[b & 1][k][e]); }
        }
    }
    ldsw128(l.tid * 32, 4096, __builtin_bit_cast(u32x4, f32x4{acc[0], acc[1], acc[2], acc[3]}));
    ldsw128(l.tid * 32, 4112, __builtin_bit_cast(u32x4, f32x4{acc[4], acc[5], acc[6], acc[7]}));
    LDS_BAR();
    float v = 0;
#pragma unroll
    for (int k = 0; k < 8; ++k) v += ldsrf(l.tid * 4, 4096 + k * 1024);
    v = rbf(v);
    if (x == 2) v = rbf(1.0f + v);
    st32(r, l.tid < 216 && 216 * j + l.tid < 640 ? (216 * j + l.tid) * 4 : OOB, W_MOD + (2 + x) * 2560, __float_as_uint(v) | 1u);
    LDS_BAR();
}
DEVI void page_touch(rsrc_t r, const int a) {
    if (T_TG == 0 || threadIdx.x < 128) return;
    constexpr unsigned G = T_TG ? T_TG * 4096 : 1, N = 96 * PAGE / G, R = (N + 2047) / 2048;
    unsigned tc[R];
#pragma unroll
    for (unsigned i = 0; i < R; ++i) { const unsigned n = i * 2048 + (threadIdx.x - 128) * 16 + a; tc[i] = ld32(r, n < N ? W_END + P_SEG + n * G + G - 128 : OOB, 0); }
    touched(tc);
}
DEVI unsigned touch_k(const Prm& p, int x, int u) { return touch_w(kvw(p, u & 1), x, (u >> 1) * 256 + threadIdx.x); }
DEVI void kv_touch(const Prm& p, const int x, const int j, const unsigned cd = 0) {
    unsigned tc[9];
#pragma unroll
    for (int i = 0; i < 9; ++i) tc[i] = i < 6 && 8 + 13 * i + j < 80 ? touch_k(p, x, 8 + 13 * i + j) : touch_c(p, x, max(231 - 13 * i - j, 120));
    touched(tc, cd);
}
template <bool ISV> DEVI void kv_role(const Prm& p, rsrc_t r, const int hp, const int ty, const Cd& cd) {
    Ln l = mkln();
    u32x4 wt[4][20];
    {
        const int row = 4 * l.w + l.g;
        if (ty == 2) { ask(r, cd); kv_touch(p, hp >> 2, 9 + (hp & 3)); }
        const unsigned tc[1] = {touch_k(p, hp >> 2, 2 * (hp & 3) + (ty & 1))};
        if (ty != 2) ask(r, cd);
        const uchar* src = kvw(p, ty) + hp * 512 + (l.c16 ^ swz1(row)) * 16 + (size_t)row * 8192;
        auto issue = [&](int qd, int i) { glds16(src + (qd & 1) * 256 + (size_t)((qd >> 1) * 10 + i) * 16 * 8192, ((qd & 3) * 10 + i) * 4096 + l.w * 1024); };
        unsigned ad[2][2];
#pragma unroll
        for (int i = 0; i < 2; ++i)
#pragma unroll
            for (int hh = 0; hh < 2; ++hh) {
                const int rw = 8 * l.g + 4 * hh + l.q4, un = ISV ? 4 * l.w + 2 * i + (l.p4 >> 1) : 4 * l.w + l.p4;
                ad[i][hh] = rw * 256 + ((un ^ swz1(rw)) * 16) + (ISV ? (l.p4 & 1) * 8 : 8 * i);
            }
        touched(tc, cd.c);
#pragma unroll
        for (int n = 0; n < 20; ++n) issue(n / 10, n % 10);
        sfor<0, 8>([&](auto qq) {
            constexpr int qd = qq;
            if (qd < 7) VM_BAR(10); else VM_BAR(0);
#pragma unroll
            for (int k = 0; k < 5; ++k)
#pragma unroll
                for (int i = 0; i < 2; ++i) {
                    if (qd < 6) issue(qd + 2, 2 * k + i);
                    wt[2 * (qd & 1) + i][5 * (qd >> 1) + k] = trd2(ad[i][0], (5 * (qd & 3) + k) * 8192, ad[i][1], (5 * (qd & 3) + k) * 8192);
                }
#pragma unroll
            for (int n = 0; n < 10; ++n)
                if (!(qd & 1) || 5 * (qd >> 1) + (n >> 1) < 12) asm volatile("" : "+a"(wt[2 * (qd & 1) + (n & 1)][5 * (qd >> 1) + (n >> 1)]));
        });
    }
    u32x4 t[10];
    gate(r, cd);
    const Pg pg = mkpg(p, wait_ge(r, l.bud, l.lane ? F_H + l.p4 * 128 : 0, l.lane ? 1 : NINIT << 8));
    act_load(r, l, W_H, t);
    unsigned ca[10], ra[4];
#pragma unroll
    for (int i = 0; i < 10; ++i) {
        const int n = i * 256 + l.tid, tok = n / 80, q = n - tok * 80, rho = 4 * (tok >> 3) + (tok & 3);
        ca[i] = ((tok >> 2) & 1) * 20480 + q * 256 + ((rho ^ (q & 15)) * 16);
        ldsw128(ca[i], 0, t[i]);
    }
    act_load(r, l, W_H + 40960, t);
#pragma unroll
    for (int k = 0; k < 4; ++k) ra[k] = l.g * 256 + ((l.c16 ^ (4 * k + l.g)) * 16);
    LDS_BAR();
    const unsigned fo = l.tid == 0 ? pg.hf + F_KV + hp * 128 + 16 + ty * 16 : OOB;
    const unsigned hs = ty == 2 ? 16384 : 32768, ho = (ty == 2 ? O_Q : ty * 16384) + 2 * hp * hs + l.w * (ISV ? 2048 : 1024);
    const unsigned vx = ISV ? (l.w & 1) * 20480 : 0;
    f32x4 acc[2][4];
    u32x4 f[5], pk[4];
    unsigned so = OOB, sv = 0;
#pragma unroll
    for (int k = 0; k < 4; ++k) { ra[k] += vx; f[k] = ldsr128(ra[k], k * 1024); acc[1][k] = f32x4{0, 0, 0, 0}; pk[k] = u32x4{0, 0, 0, 0}; }
    auto pack = [&](const int H, const int e) {
        const int i = e >> 1, o = 2 * (e & 1);
        const unsigned v = pk2(acc[H][i][o], acc[H][i][o + 1]);
        if (ISV) pk[i][2 * H + (e & 1)] = v; else pk[2 * H + (i >> 1)][e & 3] = v;
    };
    auto store = [&](const int k) { st128m(r, so, sv + (ISV ? (k >> 1) * hs + (k & 1) * 1024 : (k & 1) * hs + (k >> 1) * 4096), pk[k]); };
#pragma unroll 1
    for (int s = 0; s < 64; ++s) {
        const unsigned hn = W_H + min(s + 2, 63) * 40960;
        if (s == 2 || s == 12) wait_ge(r, l.bud, F_H + min(l.lane, 11 * s + 1) * 128, 1);
        SB();
        sfor<0, 160>([&](auto nn) {
            constexpr int n = nn, i = n & 3, m = n >> 2, H = m / 20, ks = m % 20, mh = m % 20, mn = (m + 4) % 40;
            constexpr bool ag = i < 2 || ks < 12;
            mfa<ISV, ag, ks == 0>(acc[H][i], wt[i][ks], f[m % 5]);
            if (i == 0) {
                if (mn % 20 < 4) ra[mn & 3] ^= mn < 20 ? 0x10000 | 20480 : 20480;
                f[(m + 4) % 5] = ldsr128(ra[mn & 3], (mn % 20) * 1024);
            }
            if (i == 1) {
                if (m % 3 == 2 && m < 32) { ca[m / 3] ^= 0x10000; ldsw128(ca[m / 3], 0, t[m / 3]); }
                if (m % 3 == 0 && m > 0 && m < 33) t[m / 3 - 1] = ld128(r, l.tid * 16, hn + (m / 3 - 1) * 4096);
                if (!ISV && (m == 4 || m == 7 || m == 25 || m == 28)) store(m < 20 ? m / 3 + 1 : m / 3 - 8);
                if (m == 0) BAR();
                if (m == 34) { asm volatile("s_waitcnt lgkmcnt(4)" ::: "memory"); BAR(); }
            }
            if (i == 2) {
                if (mh >= 2 && mh < 6) { pack(1 - H, 2 * mh - 4); pack(1 - H, 2 * mh - 3); }
                if (m == 19) { so = l.lane * 16; sv = pg.kv(s) + ho; }
                if ((m == 16 || m == 22) && (m == 16) == (s < 64)) {
                    asm volatile("s_waitcnt vmcnt(%0)" :: "n"(m / 3 - 2) : "memory");
                    BAR();
                    st32(r, fo, 0, s);
                }
            }
            if (i == 3 && ISV && m >= 3 && m < 7) store(m - 3);
            SB();
        });
    }
    asm volatile("s_nop 7\n\ts_nop 7" ::: "memory");
#pragma unroll
    for (int e = 0; e < 8; ++e) pack(1, e);
#pragma unroll
    for (int k = ISV ? 0 : 2; k < 4; ++k) store(k);
    VM_BAR(0);
    st32(r, fo, 0, 64);
}

constexpr int S_TL = 54, RING = 8;
DEVI int h2set(int s) { return (s < S_TL ? s % 3 : 3) * 2048; }
constexpr unsigned LQ_O = 0, LQ_KM = 16384, LQ_X = 24576;
DEVI void ao_role(const Prm& p, rsrc_t r, const int hp, const int j) {
    Ln l = mkln();
    u32x4 wo[5][2][8];
    load_slab2<4>(l, p.Wo + hp * 256 * 1280, wo, 0);
#pragma unroll
    for (int i = 0; i < 40; ++i) asm volatile("" : "+a"(wo[i >> 3][(i >> 2) & 1][i & 3]));
    load_slab2<4>(l, p.Wo + (hp * 256 + 128) * 1280, wo, 4);
#pragma unroll
    for (int i = 0; i < 24; ++i) asm volatile("" : "+a"(wo[i >> 3][(i >> 2) & 1][4 + (i & 3)]));
    const int Tw = l.w & 1, hh = l.w >> 1, h = 2 * hp + hh;
    const int tokc = 8 * (l.c16 >> 2) + 4 * Tw + (l.c16 & 3);
    const unsigned tg = (4 * l.q4 + (l.p4 >> 1)) * UN + hp * 256 + l.g * 32 + (l.p4 & 1) * 16;
    const Pg pg = mkpg(p, wait_ge(r, l.bud, 0, NINIT << 8));
    const unsigned fo = l.tid == 0 ? pg.hw + F_OP + j * 2048 + hp * 128 : OOB, fm = pg.hf + F_M + (2 * h + (l.lane & 1)) * 128, fk = pg.hf + F_KV + hp * 128 + 16 + l.p4 * 16;
    const unsigned fg = l.w == 0 ? pg.hw + F_H2 + (l.lane >> 2) * 128 + l.p4 * 4 : BIG;
    const unsigned xa = LQ_X + hh * 16384 + l.lane * 16, xw = xa + Tw * 4096;
    unsigned kvn = 0, mn = 0;
    auto fetch = [&](unsigned sp, int i) { return ld128(r, l.lane * 16 + (i & 3) * 1024, sp + (i >= 8 ? O_Q + h * 16384 : h * 32768 + (i >> 2) * 16384) + Tw * 4096); };
    wait2(r, l, kvn, fk, j + 1);
    u32x4 qf[4];
#pragma unroll
    for (int i = 0; i < 12; ++i) { const u32x4 v = fetch(pg.kv(j), i); if (i < 8) ldsw128(xw, (i >> 2) * 8192 + (i & 3) * 1024, v); else qf[i & 3] = v; }
    asm volatile("" : "+v"(qf[0]), "+v"(qf[1]), "+v"(qf[2]), "+v"(qf[3]));
#pragma unroll 1
    for (int it = 0; it < 32; ++it) {
        const int s = 2 * it + j;
        constexpr float CE = 0.08838834764831845f * 1.4426950408889634f;
        LDS_BAR();
        unsigned mf = ld32(r, l.lane < 4 ? fm : fk, 0);
        const unsigned fs = fg + h2set(max(s - RING, 0)), gf = ld32(r, fs, 0);
        f32x4 ot[8];
        float mt, st;
        {
            f32x4 sc[2];
            u32x4 kf[8], vf[8];
#pragma unroll
            for (int k = 0; k < 8; ++k) kf[k] = ldsr128(xa, (k & 1) * 4096 + (k >> 1) * 1024);
#pragma unroll
            for (int i = 0; i < 8; ++i) vf[i] = ldsr128(xa, 8192 + i * 1024);
            SB();
            sfor<0, 8>([&](auto kk) { constexpr int k = kk; mfa<false, false, k < 2>(sc[k & 1], kf[k], qf[k >> 1]); });
            asm volatile("s_nop 7" : "+v"(sc[0]), "+v"(sc[1]));
            mt = -3.0e38f;
#pragma unroll
            for (int T = 0; T < 2; ++T)
#pragma unroll
                for (int e = 0; e < 4; ++e) { const float v = (8 * l.g + 4 * T + e <= tokc) ? sc[T][e] * CE : -1.4426950408889634e9f; sc[T][e] = v; mt = fmaxf(mt, v); }
            mt = xmax4(mt);
#pragma unroll
            for (int T = 0; T < 2; ++T)
#pragma unroll
                for (int e = 0; e < 4; ++e) sc[T][e] = __builtin_amdgcn_exp2f(sc[T][e] - mt);
            u32x4 pt = pk8(sc[0], sc[1]), ps = {pt[2], pt[3], pt[0], pt[1]};
            asm volatile("s_nop 1" : "+v"(pt), "+v"(ps));
            sfor<0, 8>([&](auto ii) { constexpr int i = ii; mfa<false, false, true>(ot[i], vf[i], (i & 2) ? ps : pt); });
            st = 0;
#pragma unroll
            for (int e = 0; e < 4; ++e) st += blo(pt[e]) + bhi(pt[e]);
            st = xsum4(st);
        }
        VM_BAR(0);
        st32(r, fo, 0, it);
        mf = opq(mf);
        if (l.w == 0 && __builtin_amdgcn_ballot_w64(opq(gf) + RING <= s) != 0) wait_ge(r, l.bud, fs, s + 1 - RING);
        mn = min(__builtin_amdgcn_readlane(mf, 0), __builtin_amdgcn_readlane(mf, 1));
        kvn = min(min(__builtin_amdgcn_readlane(mf, 4), __builtin_amdgcn_readlane(mf, 5)), __builtin_amdgcn_readlane(mf, 6));
        wait2(r, l, mn, fm, s + 1);
        const int sn = min(s + 2, 62 + j);
        {
            u32x2 vm[8];
            const unsigned mo = pg.kv(s) + O_KVM + h * 16384;
            const u32x4 k0 = ld128(r, l.lane * 16, mo + Tw * 4096), k1 = ld128(r, l.lane * 16 + 1024, mo + Tw * 4096);
#pragma unroll
            for (int i = 0; i < 8; ++i) vm[i] = ld64(r, l.c16 * 16 + (l.g & 1) * 8 + 2048 + (i & 3) * 256, mo + (i >> 2) * 4096);
            SB();
            ldsw128(l.w * 2048 + l.lane * 16, LQ_KM, k0); ldsw128(l.w * 2048 + l.lane * 16, LQ_KM + 1024, k1);
            const unsigned ka = LQ_KM + l.w * 2048 + (8 * l.g + l.q4) * 16 + (l.p4 & 1) * 8;
            u32x4 km[4];
            asm volatile("" ::: "memory");
#pragma unroll
            for (int k = 0; k < 4; ++k) km[k] = trd2(ka, k * 512, ka, k * 512 + 64);
            SB();
            f32x4 sm;
            sfor<0, 4>([&](auto kk) { constexpr int k = kk; mfa<false, false, k == 0>(sm, km[k], qf[k]); });
            asm volatile("s_nop 7" : "+v"(sm));
            sm *= CE;
            const float mx = fmaxf(mt, xmax4(fmaxf(fmaxf(sm[0], sm[1]), fmaxf(sm[2], sm[3])))), al = __builtin_amdgcn_exp2f(mt - mx);
#pragma unroll
            for (int e = 0; e < 4; ++e) sm[e] = l.g < 2 ? __builtin_amdgcn_exp2f(sm[e] - mx) : 0.f;
            const float inv = __builtin_amdgcn_rcpf(al * st + xsum4((sm[0] + sm[1]) + (sm[2] + sm[3]))), be = al * inv;
            const u32x2 pm = {pk2(sm[0] * inv, sm[1] * inv), pk2(sm[2] * inv, sm[3] * inv)};
            SB();
#pragma unroll
            for (int i = 0; i < 13; ++i) {
                if (i < 8) ot[i] *= be;
                if (i == 8) asm volatile("s_nop 1");
                SB();
                if (i >= 1 && i < 9) asm volatile("v_mfma_f32_16x16x16_bf16 %0, %1, %2, %0" : "+v"(ot[i - 1]) : "v"(vm[i - 1]), "v"(pm));
                if (i == 8) asm volatile("s_nop 3");
                if (i >= 6 && !(i & 1)) ldsw128(Tw * 8192 + hh * 4096 + l.lane * 16, LQ_O + (i - 6) * 512, pk8(ot[i - 6], ot[i - 5]));
                SB();
            }
        }
        wait2(r, l, kvn, fk, sn + 1);
        LDS_BAR();
        const unsigned ob = pg.un(s & (RING - 1)), np = pg.kv(sn);
        {
            u32x4 of[2][8], pk, pf[8];
#pragma unroll
            for (int i = 0; i < 8; ++i) of[0][i] = ldsr128(l.lane * 16, LQ_O + i * 1024);
            f32x4 y[2][2];
            SB();
            sfor<0, 171>([&](auto nn) {
                constexpr int n = nn, G = n >> 4, m = n & 15, k = m >> 1, i = m & 1, T = G / 5, P = G % 5, M = G - 1, MT = M / 5, MP = M % 5;
                if constexpr (G < 10) mfa<false, k < 4 || P < 3, k == 0>(y[G & 1][i], wo[P][i][k], of[T][k]);
                if (n == 160) asm volatile("s_nop 7");
                if (n == 170) asm volatile("s_nop 1" :: "v"(pk));
                if (n == 152) asm volatile("" : "+v"(qf[0]), "+v"(qf[1]), "+v"(qf[2]), "+v"(qf[3]));
                if (M >= 0 && m >= 4 && m < 8) pk[m & 3] = pk2(y[M & 1][(m >> 1) & 1][2 * (m & 1)], y[M & 1][(m >> 1) & 1][2 * (m & 1) + 1]);
                if (M >= 0 && m == 9) st128m(r, tg, ob + 2 * MT * UN + (MP < 4 ? l.w * 16384 + (MP >> 1) * 4096 + (MP & 1) * 128 : 65536 + (l.w >> 1) * 4096 + (l.w & 1) * 128), pk);
                if (G < 4 && (m == 2 || m == 10)) of[1][2 * G + (m >> 3)] = ldsr128(l.lane * 16, LQ_O + 8192 + (2 * G + (m >> 3)) * 1024);
                if (G < 6 && (m == 1 || m == 13)) { const u32x4 v = fetch(np, 2 * G + (m >> 3)); if (G < 4) pf[(2 * G + (m >> 3)) & 7] = v; else qf[(2 * G + (m >> 3)) & 3] = v; }
                if (G >= 6 && G < 10 && (m == 1 || m == 13)) ldsw128(xw, ((G - 6) >> 1) * 8192 + ((2 * G + (m >> 3)) & 3) * 1024, pf[(2 * G - 12 + (m >> 3)) & 7]);
                SB();
            });
        }
    }
    asm volatile(".Lk2:");
    VM_BAR(0);
    st32(r, fo, 0, 32);
}

DEVI void mksel(const Ln& l, u32x4 (&sel)[2]) {
#pragma unroll
    for (int n = 0; n < 4; ++n) {
        const unsigned v = ((l.c16 >> 1) & 3) == n ? ((l.c16 & 1) ? 0x3F800000u : 0x00003F80u) : 0u;
        sel[0][n] = l.c16 < 8 ? v : 0u; sel[1][n] = l.c16 < 8 ? 0u : v;
    }
}
DEVI float tsum(float x) { x += dpp<0x4e>(x); x += dpp<0x124>(x); x += dpp<0x128>(x); return xsum4(x); }
DEVI void r1_role(const Prm& p, rsrc_t r, const int k, const int n0) {
    Ln l = mkln();
    u32x4 sel[2];
    mksel(l, sel);
    const bool act = (l.lane & 40) == 0;
    const unsigned cl = 8 * ((l.c16 >> 1) & 3) + 4 * (l.g & 1), col[2] = {128 * l.w + 64 * (l.g >> 1) + 32 * (l.c16 >> 3) + cl, 512 + 32 * l.w + cl};
    unsigned mo[6];
#pragma unroll
    for (int c = 0; c < 6; ++c) mo[c] = (1280 + 640 * (c >> 1) + col[c & 1]) * 4;
    u32x4 md[6];
    wait_mod<6>(r, l, mo, md);
    const Pg pg = mkpg(p, wait_ge(r, l.bud, 0, NINIT << 8));
    const unsigned rb = (17 - (l.g & 1)) * UN + l.w * 128 + (l.lane & 7) * 16;
    const unsigned ro[2] = {rb + (2 * (l.g >> 1) + (l.c16 >> 3)) * 16384, act ? rb + 65536 : OOB};
    const unsigned fo = l.lane == 0 ? pg.hw + F_H2 + l.w * 4 : OOB;
    const unsigned v2 = (l.lane >> 3) * 256 + (l.w & 1) * 128 + (l.lane & 7) * 16, fi = pg.hw + F_OP + l.c16 * 128;
    u32x4 t[10]; u32x2 xr[2];
#pragma unroll
    for (int i = 0; i < 10; ++i) asm volatile("" : "=v"(t[i]));
    auto part = [&](int n, unsigned va, unsigned vb, int i0, int i1) {
        const unsigned ob = pg.un((n >> 4) & (RING - 1)) + (n & 15) * UN;
        SB();
#pragma unroll
        for (int i = i0; i < i1; ++i)
            asm volatile("buffer_load_dwordx4 %0, %1, %2, %3 offen offset:%4 sc1" : "+v"(t[i]) : "v"(i < 8 ? va : vb), "s"(r),
                         "s"(i < 8 ? ob + l.w * 16384 + (i >> 2) * 4096 : ob + 65536 + (l.w >> 1) * 4096), "n"(i < 8 ? (i & 3) * 1024 : (i & 1) * 2048) : "memory");
        SB();
    };
    auto issue = [&](int n) { part(n, l.lane * 16, v2, 0, 10); };
    auto ldx = [&](int n) {
#pragma unroll
        for (int c = 0; c < 2; ++c) asm volatile("global_load_dwordx2 %0, %1, %2" : "+v"(xr[c]) : "v"((l.lane & 1) * 1280 + col[c] * 2), "s"(p.x + (size_t)n * 2560) : "memory");
    };
    bool ready = false, early = false;
    unsigned sa = (l.lane & 1) * 32, nf = 0, po = 0, pv = 0;
    const unsigned f2 = fi + (l.g >> 1) * 2048;
    auto seen = [&](int n) { return n < 1024 && (unsigned)(__builtin_amdgcn_ballot_w64(nf < (n >> 5) + 1) >> (2 * (n & 16))) == 0; };
    asm volatile("" : "=v"(xr[0]), "=v"(xr[1]));
    ldx(n0);
    wg_init(l, 256);
#pragma unroll 1
    for (int n = n0, nn; n < 1024; n = nn) {
        const int s = n >> 4, pq = (n & 2) * 4 + ((n >> 1) & 6) + (n & 1);
        nn = n < 16 * S_TL ? n + 12 : n + 16;
        wait_wg(r, l, ready, fi + (s & 1) * 2048, (s >> 1) + 1, 256, n + 1, n == n0);
        if (!ready) { issue(n); st32(r, OOB, 0, 0); }
        if (!early) st32(r, OOB, 4, 0);
        SB();
        f32x4 a[2];
        sfor<0, 10>([&](auto ii) {
            constexpr int i = ii;
            asm volatile("s_waitcnt vmcnt(%1)" : "+v"(t[i]) : "n"(11 - i));
            mfa<false, false, (i & 7) == 0>(a[i >> 3], sel[(i >> 2) & 1], t[i]);
        });
        asm volatile("s_nop 7" : "+v"(a[0]), "+v"(a[1]), "+v"(xr[0]), "+v"(xr[1]), "+v"(nf));
#pragma unroll
        for (int e = 0; e < 4; ++e) a[1][e] += dpp<0x128>(a[1][e]);
        f32x4 x1[2]; u32x2 u[2]; float sum = 0;
#pragma unroll
        for (int c = 0; c < 2; ++c) {
            const f32x4 ga = __builtin_bit_cast(f32x4, md[c]);
            u[c] = u32x2{pk2(blo(xr[c][0]) + ga[0] * a[c][0], bhi(xr[c][0]) + ga[1] * a[c][1]), pk2(blo(xr[c][1]) + ga[2] * a[c][2], bhi(xr[c][1]) + ga[3] * a[c][3])};
            if (c == 1 && !act) u[c] = u32x2{0, 0};
            x1[c] = f32x4{blo(u[c][0]), bhi(u[c][0]), blo(u[c][1]), bhi(u[c][1])};
            sum += (x1[c][0] + x1[c][1]) + (x1[c][2] + x1[c][3]);
        }
        const unsigned df = early ? fo : OOB;
        early = (nn >> 4) == s || seen(nn);
        const unsigned ea = early ? l.lane * 16 : BIG, eb = early ? v2 : BIG;
        asm volatile("" : "+v"(sum), "+v"(u[0]), "+v"(u[1]));
        ldx(min(nn, 1023));
        part(nn, ea, eb, 0, 3);
        const float mw = tsum(sum) * (1.0f / 160);
        part(nn, ea, eb, 3, 5);
        asm volatile("buffer_load_dword %0, %1, %2, 0 offen sc1" : "+v"(nf) : "v"(f2), "s"(r) : "memory");
        float var = 0;
#pragma unroll
        for (int c = 0; c < 2; ++c) {
            x1[c] -= mw;
            if (c == 1 && !act) x1[c] = f32x4{0, 0, 0, 0};
            var += (x1[c][0] * x1[c][0] + x1[c][1] * x1[c][1]) + (x1[c][2] * x1[c][2] + x1[c][3] * x1[c][3]);
        }
        part(nn, ea, eb, 5, 7);
        var = tsum(var);
        sa ^= 64;
        if (l.lane < 2) ldsw64(sa + l.w * 8, 0, u32x2{__float_as_uint(mw), __float_as_uint(var)});
        part(nn, ea, eb, 7, 9);
        asm volatile("s_waitcnt vmcnt(12)" ::: "memory");
        st32(r, df, po, pv);
        LDS_BAR();
        const f32x4 s0 = __builtin_bit_cast(f32x4, ldsr128(sa, 0)), s1 = __builtin_bit_cast(f32x4, ldsr128(sa, 16));
        part(nn, ea, eb, 9, 10);
        const float mean = ((s0[0] + s0[2]) + (s1[0] + s1[2])) * 0.25f;
        const float e0 = s0[0] - mean, e1 = s0[2] - mean, e2 = s1[0] - mean, e3 = s1[2] - mean;
        const float rstd = __builtin_amdgcn_rsqf(((s0[1] + s0[3]) + (s1[1] + s1[3])) * (1.0f / 640) + ((e0 * e0 + e1 * e1) + (e2 * e2 + e3 * e3)) * 0.25f + 1e-5f);
        const float dm = mw - mean;
#pragma unroll
        for (int c = 0; c < 2; ++c) {
            const f32x4 hv = (x1[c] + dm) * rstd * __builtin_bit_cast(f32x4, md[4 + c]) + __builtin_bit_cast(f32x4, md[2 + c]);
            const auto w0 = __builtin_amdgcn_permlane16_swap(pk2(hv[0], hv[1]), u[c][0], false, false);
            const auto w1 = __builtin_amdgcn_permlane16_swap(pk2(hv[2], hv[3]), u[c][1], false, false);
            st128(r, ro[c], pg.un(s) + pq * 512, u32x4{w0[0], w1[0], w0[1], w1[1]});
        }
        asm volatile(".Lk4:" ::: "memory");
        po = h2set(s) + pq * 128; pv = s + 1;
        ready = early;
        if (!early) {
            asm volatile("s_waitcnt vmcnt(2)" : "+v"(nf) :: "memory");
            ready = seen(nn);
            if (ready) { issue(nn); asm volatile("s_waitcnt vmcnt(10)" ::: "memory"); } else VM_WAIT0();
            st32(r, fo, po, pv);
        }
    }
}

constexpr unsigned LF_A = 20480, LF_W = LF_A + 3088;
DEVI void ffn_role(const Prm& p, rsrc_t r, const int f, const int j) {
    Ln l = mkln();
    u32x4 wgu[3][20], wd[5][2][3];
    if (f < 16) {
        unsigned tc[6];
#pragma unroll
        for (int i = 0; i < 6; ++i) {
            const int n = min(((i & 1) * 8 + 2 * (f & 3) + j) * 256 + l.tid, 3839), row = n / 6;
            tc[i] = touch(i < 4 ? ((i & 2) ? p.Wu : p.Wg) + row * 3456 + (f >> 2) * 768 + (n - 6 * row) * 128 : p.Wd + (f >> 2) * 491520 + n * 128);
        }
        touched(tc);
    }
    {
        const int un[2] = {2 * l.w + (l.p4 >> 1), 8 + 2 * l.w + (l.p4 >> 1)}, by[2] = {(l.p4 & 1) * 8, (l.p4 & 1) * 8};
        const int row = 8 * l.w + (l.lane >> 3), u = (l.lane & 7) ^ (((row >> 1) & 1) << 2);
        load_slab1(l, p.Wg + f * 192, p.Wu + f * 192, 3456, un, by, wgu, (u < 4 ? p.Wg : p.Wu) + f * 192 + 128 + (u & 3) * 16 + (size_t)row * 3456);
        pin_a(wgu[0]); pin_a(wgu[1]);
        VM_BAR(0);
        const unsigned ad = (8 * l.g + l.q4) * 128 + (((4 * (l.p4 >> 1) + l.w) ^ ((l.q4 >> 1) << 2)) * 16) + (l.p4 & 1) * 8;
#pragma unroll
        for (int ks = 0; ks < 20; ++ks) wgu[2][ks] = trd2(ad, ks * 4096, ad, ks * 4096 + 512);
        LDS_BAR();
        pin_a(wgu[2]);
        load_slab2<3>(l, p.Wd + f * 96 * 1280, wd, 0);
    }
    if (blockIdx.x == B_Z) echo(r, SPIN, ld128(r, l.tid * 128, G_ACK));
    const Pg pg = mkpg(p, wait_ge(r, l.bud, 0, NINIT << 8));
    const unsigned fo = l.tid == 0 ? pg.hw + F_FP + j * 4096 + f * 128 : OOB, fi = pg.hw + F_H2 + j * 1024 + (l.lane & 28) * 32 + l.p4 * 4;
    const unsigned aw = LF_A + (l.w >> 1) * 1024 + l.lane * 16 + (l.w & 1) * 8;
    const unsigned am = LF_A + 2048 + ((l.w & 1) * 32 + (l.lane & 31)) * 16 + (l.w >> 1) * 8 + (l.g >> 1) * 4;
    const unsigned gc = (l.lane >> 1) & 3, wc = (l.lane >> 3) & 3;
    const unsigned cw = gc * 256 + (((4 * l.w + 2 * (l.lane >> 5) + (l.lane & 1)) ^ (2 * gc) ^ (8 * (wc & 1))) * 16), ca[2] = {cw + wc * 4096, cw + 16384 + wc * 1024};
    const unsigned ra[2] = {l.g * 256u + ((l.c16 ^ (2 * l.g)) * 16), l.g * 256u + ((l.c16 ^ (2 * l.g) ^ 8) * 16)};
    const unsigned so = j * 4096 + l.q4 * 1024 + l.w * 256 + l.g * 64 + l.p4 * 16;
    auto issue = [&](u32x4 (&t)[5], int s) {
#pragma unroll
        for (int i = 0; i < 5; ++i) t[i] = ld128(r, l.tid * 16, pg.un(s) + 17 * UN + j * 4096 + i * 16384);
    };
    auto frag = [&](const int ks) { return ldsr128(ra[(ks < 16 ? ks >> 2 : ks) & 1], ks * 1024); };
    auto put = [&](const u32x4 (&t)[5]) {
#pragma unroll
        for (int i = 0; i < 5; ++i) ldsw128(ca[i == 4], (i & 3) * 1024, t[i]);
    };
    bool early = false;
    const unsigned fe = l.w == 0 ? fi : OOB;
    wg_init(l, LF_W);
    { u32x4 t[5]; wait_wg(r, l, false, fi + h2set(0), 1, LF_W, 1); issue(t, 0); put(t); }
#pragma unroll 1
    for (int s = 0;; ++s) {
        LDS_BAR();
        const unsigned fb = (f < 6 ? pg.un(s) + 18 * UN : pg.fp(s)) + f * UN;
        f32x4 acc[3], y[2][2];
        u32x4 fr[5], af[3], pk, te[5], t[5];
        unsigned nf;
#pragma unroll
        for (int k = 0; k < 4; ++k) fr[k] = frag(k);
        SB();
        sfor<0, 60>([&](auto nn) {
            constexpr int n = nn, i = n % 3, ks = n / 3;
            mfa<false, true, ks == 0>(acc[(i + 2) % 3], wgu[(i + 2) % 3][ks], fr[ks % 5]);
            if (i == 0 && ks + 4 < 20) fr[(ks + 4) % 5] = frag(ks + 4);
            if (n == 16) nf = ld32(r, fe, h2set(s + 1));
            SB();
        });
        asm volatile("s_nop 1" : "+v"(acc[2]));
        {
            float g[6], u[6];
#pragma unroll
            for (int n = 0; n < 2; ++n) {
                auto sw = __builtin_amdgcn_permlane32_swap(__float_as_uint(acc[2][n]), __float_as_uint(acc[2][n + 2]), false, false);
                g[n] = __uint_as_float(sw[0]); u[n] = __uint_as_float(sw[1]);
            }
#pragma unroll
            for (int n = 0; n < 6; ++n) {
                if (n == 2) asm volatile("s_nop 1" : "+v"(acc[0]), "+v"(acc[1]), "+v"(g[0]), "+v"(g[1]));
                if (n >= 2) { g[n] = acc[0][n - 2]; u[n] = acc[1][n - 2]; }
                u[n] *= g[n]; g[n] = __builtin_amdgcn_exp2f(-1.4426950408889634f * g[n]);
            }
#pragma unroll
            for (int n = 0; n < 6; ++n) u[n] *= __builtin_amdgcn_rcpf(1.0f + g[n]);
            *LP(unsigned, am, 0) = pk2(u[0], u[1]);
            ldsw64(aw, 0, u32x2{pk2(u[2], u[3]), pk2(u[4], u[5])});
        }
        *LP(unsigned, l.w * 4, LF_A + 3072) = s < 63 && __builtin_amdgcn_ballot_w64(nf < s + 2) == 0;
        VM_BAR(0);
        nf = *LP(unsigned, 0, LF_A + 3072);
#pragma unroll
        for (int k = 0; k < 3; ++k) af[k] = ldsr128(l.lane * 16, LF_A + k * 1024);
        const unsigned fl = early ? fo : OOB;
        early = __builtin_amdgcn_readfirstlane(nf);
        if (early) issue(te, s + 1);
        st32(r, fl, 0, s);
        nf = ld32(r, early ? OOB : fi, h2set(s + 1));
        SB();
        sfor<0, 36>([&](auto nn) {
            constexpr int q = nn, G = q / 6, m = q % 6, k = m >> 1, i = m & 1, M = G - 1;
            if constexpr (G < 5) mfa<false, false, k == 0>(y[G & 1][i], wd[G][i][k], af[k]);
            if (q == 30) asm volatile("s_nop 7");
            if (M >= 0 && (m == 2 || m == 3)) { pk[2 * i] = pk2(y[M & 1][i][0], y[M & 1][i][1]); pk[2 * i + 1] = pk2(y[M & 1][i][2], y[M & 1][i][3]); }
            if (M >= 0 && m == 4) st128m(r, so, fb + M * 16384, pk);
            if (q == 35) asm volatile("s_nop 1" :: "v"(pk));
            SB();
        });
        asm volatile(".Lk3:");
        if (early) { put(te); asm volatile("; a" ::: "memory"); continue; }
        if (s < 63 && __builtin_amdgcn_ballot_w64(nf < s + 2) == 0) {
            wait_wg(r, l, true, 0, 0, LF_W, s + 2);
            issue(t, s + 1);
            VM_BAR(5);
            st32(r, fo, 0, s + 1);
            put(t);
            asm volatile("; b" ::: "memory");
            continue;
        }
        VM_BAR(0);
        st32(r, fo, 0, s + 1);
        if (s == 63) break;
        wait_wg(r, l, false, fi + h2set(s + 1), s + 2, LF_W, s + 2);
        issue(t, s + 1);
        put(t);
    }
}

DEVI void r2_role(const Prm& p, rsrc_t r, const int k) {
    Ln l = mkln();
    u32x4 sel[2];
    mksel(l, sel);
    const int n0 = (k + 40 - 10 * l.w) % 40, rr = n0 & 7;
    const bool act = l.g < 2;
    unsigned mo[3], xo[3];
#pragma unroll
    for (int c = 0; c < 3; ++c) {
        const unsigned col = (c < 2 ? 128 * l.w + 64 * c + 32 * (l.g >> 1) : 512 + 32 * l.w) + 8 * l.q4 + 4 * (l.g & 1);
        mo[c] = (3200 + col) * 4;
        xo[c] = c < 2 || act ? (4 * rr + l.p4) * 1280 + col * 2 : OOB;
    }
    u32x4 md[3];
    wait_mod<3>(r, l, mo, md);
    const Pg pg = mkpg(p, wait_ge(r, l.bud, 0, NINIT << 8));
    const rsrc_t ru = __builtin_amdgcn_make_buffer_rsrc(p.out, 0, ROWS_B, 0x00020000);
    const unsigned to = (rr & 1) * 4096 + (rr >> 1) * 1024, pv = to + l.w * 256 + l.c16 * 16 + l.g * UN, pv2 = act ? pv : OOB;
    const unsigned xv = to + (l.p4 >> 1) * 512 + l.w * 128 + l.q4 * 32 + (l.p4 & 1) * 16 + (l.g >> 1) * 16384 + (l.g & 1) * 8, xv2 = act ? xv : OOB;
    const unsigned fl = pg.hw + F_FP + (rr & 1) * 4096 + (l.lane < 18 ? l.lane : 0) * 128;
    unsigned kn = 0;
#pragma unroll 1
    for (int s = n0 >> 3; s < 64; s += 5) {
        const unsigned ub = pg.un(s), fa = ub + 18 * UN, fb = pg.fp(s);
        if (__builtin_amdgcn_ballot_w64(kn < s + 1) != 0) {
            kn = wait_ge(r, l.bud, fl, max(s - 1, 0), 1);
            if (__builtin_amdgcn_ballot_w64(kn < s + 1) != 0) kn = wait_ge(r, l.bud, fl, s + 1);
        } else if (0) __builtin_amdgcn_s_sleep(0);
        u32x4 t[25]; u32x2 xr[3];
#pragma unroll
        for (int i = 0; i < 25; ++i) t[i] = ld128(r, i % 5 == 1 ? pv2 : pv, (i % 5 < 2 ? fa + (i % 5) * 4 * UN : fb + (4 * (i % 5) - 2) * UN) + (i / 5) * 16384);
#pragma unroll
        for (int c = 0; c < 3; ++c) xr[c] = ld64(r, c < 2 ? xv : xv2, ub + 16 * UN + c * 32768);
        SB();
        f32x4 a[3];
        sfor<0, 25>([&](auto ii) { constexpr int i = ii; mfa<false, false, i % 10 == 0>(a[i / 10], sel[(i / 5) & 1], t[i]); });
        asm volatile(".Lk5:\n\ts_nop 7" : "+v"(a[0]), "+v"(a[1]), "+v"(a[2]));
#pragma unroll
        for (int c = 0; c < 3; ++c) {
            const f32x4 gb = __builtin_bit_cast(f32x4, md[c]);
            const u32x2 o = {pk2(blo(xr[c][0]) + gb[0] * a[c][0], bhi(xr[c][0]) + gb[1] * a[c][1]), pk2(blo(xr[c][1]) + gb[2] * a[c][2], bhi(xr[c][1]) + gb[3] * a[c][3])};
            __builtin_amdgcn_raw_buffer_store_b64(o, ru, xo[c], s * 40960, 0);
        }
    }
}

DEVI void ln1_role(const Prm& p, rsrc_t r, const int x, const int j, const Cd& cd) {
    Ln l = mkln();
    const int a = 4 * j + x, row = 32 * a + 4 * l.w + l.g;
    u32x4 xv[2][5];
    {
        const int n = min(l.tid, 199), rw = n / 5;
        const unsigned tc[1] = {touch(l.tid < 246 ? p.wmod + (40 * j + rw) * 7680 + x * 640 + (n - 5 * rw) * 128 : p.c + (l.tid - 246) * 128)};
        if (j >= 6) {
            u32x4 wm[10]; unsigned cc[10], tk[4];
            ask(r, cd);
            touched(tc);
            mod_load(p, l, 10 * x + j - 6, wm, cc);
            mod_block(r, l, 10 * x + j - 6, wm, cc, cd);
#pragma unroll
            for (int i = 0; i < 4; ++i) tk[i] = touch_w(p.Wq, x, 2560 * i + (j - 6) * 256 + l.tid);
            touched(tk);
        } else if (j) kv_touch(p, x, j - 1);
#pragma unroll
        for (int k = 0; k < 10; ++k) xv[k / 5][k % 5] = *(const GAS u32x4*)(p.x + (size_t)(row + 16 * (k / 5)) * 1280 + (l.c16 + 16 * (k % 5)) * 16);
        if (j < 6) ask(r, cd);
        touched(tc, cd.c);
    }
    float mean[2], rstd[2];
#pragma unroll
    for (int i = 0; i < 2; ++i) {
        float sum = 0, var = 0;
#pragma unroll
        for (int k = 0; k < 20; ++k) sum += blo(xv[i][k >> 2][k & 3]) + bhi(xv[i][k >> 2][k & 3]);
        mean[i] = rsum16(sum) * (1.0f / 640);
#pragma unroll
        for (int k = 0; k < 20; ++k) { const float u = blo(xv[i][k >> 2][k & 3]) - mean[i], v = bhi(xv[i][k >> 2][k & 3]) - mean[i]; var += u * u + v * v; }
        rstd[i] = __builtin_amdgcn_rsqf(rsum16(var) * (1.0f / 640) + 1e-5f);
    }
    {
        const unsigned mo[2] = {l.tid * 16u, 4096u + l.w * 256 + l.c16 * 16};
        u32x4 md[2];
        if (j < 6) gate(r, cd);
        wait_mod<2>(r, l, mo, md);
        ldsw128(mo[0], 1024, md[0]); ldsw128(mo[1], 1024, md[1]);
    }
    LDS_BAR();
#pragma unroll
    for (int k = 0; k < 5; ++k) {
        f32x4 sa[2], ca[2];
#pragma unroll
        for (int i = 0; i < 2; ++i) {
            sa[i] = __builtin_bit_cast(f32x4, ldsr128(l.c16 * 32, 1024 + k * 512 + i * 16));
            ca[i] = __builtin_bit_cast(f32x4, ldsr128(l.c16 * 32, 1024 + 2560 + k * 512 + i * 16));
        }
#pragma unroll
        for (int i = 0; i < 2; ++i) {
            u32x4 o;
#pragma unroll
            for (int e = 0; e < 4; ++e)
                o[e] = pk2((blo(xv[i][k][e]) - mean[i]) * rstd[i] * ca[e >> 1][(2 * e) & 3] + sa[e >> 1][(2 * e) & 3],
                           (bhi(xv[i][k][e]) - mean[i]) * rstd[i] * ca[e >> 1][(2 * e + 1) & 3] + sa[e >> 1][(2 * e + 1) & 3]);
            st128(r, (row + 16 * i) * 1280 + (l.c16 + 16 * k) * 16, W_H, o);
        }
    }
    VM_BAR(0);
    if (l.tid == 0) st32(r, F_H + a * 128, 0, 1);
}
DEVI void init_role(const Prm& p, rsrc_t r, const int x, const int j, const Cd& cd) {
    Ln l = mkln();
    if (j == 15) {
        const rsrc_t rc = __builtin_amdgcn_make_buffer_rsrc(p.comm, 0, 4 * PAGE, 0x00020000);
        const int a = 4 * x + l.w;
        const u32x4 sv = {SENT, SENT, SENT, SENT};
        u32x4 v = sv;
#pragma unroll
        for (int i = 0; i < 4; ++i) st128(r, l.tid * 16, F_KV + (4 * x + i) * 4096, u32x4{0, 0, 0, 0});
        if (a < 10) {
            v = *(const GAS u32x4*)(p.mem0 + (2 * (l.c16 >> 2) + (l.c16 & 1)) * 1280 + a * 128 + (2 * l.g + ((l.c16 >> 1) & 1)) * 16);
#pragma unroll
            for (int e = 0; e < 4; ++e) v[e] = min(v[e], 0xFFFFFFFEu);
        }
#pragma unroll
        for (int i = 3; i >= 0; --i) st128(rc, l.lane * 16, 3 * PAGE + PH(a * 1024) + (i >> 1) * 32768 + (i & 1) * 8192, i < 2 ? v : sv);
        ask(r, cd);
        VM_BAR(0);
        if (l.w) gate(r, cd);
        if (l.tid == 0) {
            unsigned best[2] = {1u << 30, 1u << 30};
            for (int rep = 0; rep < 8; ++rep) {
                gu32* f = (gu32*)(p.comm + PAGE - 16384 + x * 256 + (rep & 1) * 8192);
                const unsigned c0 = now32();
                __hip_atomic_store(f, SENT, RLX_AGENT); VM_WAIT0();
                const unsigned d = now32() - c0;
                if (rep & 1) best[1] = min(best[1], d); else best[0] = min(best[0], d);
            }
            gate(r, cd);
            __hip_atomic_fetch_add((gu32*)p.ws, 256u + (((best[1] > best[0]) ^ p.flip) & 1) * (8 + (x == 0)), RLX_AGENT);
        }
    } else {
        constexpr int N = C_RND + (159 - 31 * C_RND) / 15 + 1;
        unsigned tc[N];
#pragma unroll
        for (int i = 0; i < N; ++i) tc[i] = touch_c(p, x, i < C_RND ? 31 * i + 16 + j : min(16 * C_RND + 15 * i + j, 159));
        ask(r, cd);
        touched(tc);
        gate(r, cd);
    }
    asm volatile("" :: "v"(cd.c));
}

constexpr unsigned LIM = 2048;
DEVI unsigned rt18() { unsigned long long t; asm volatile("s_memrealtime %0\n\ts_waitcnt lgkmcnt(0)" : "=s"(t) :: "memory"); return (unsigned)t << 18; }
DEVI void until(unsigned target) {
    const unsigned base = target - (LIM << 18);
    unsigned d;
#pragma unroll 1
    do d = rt18() - base; while (d < (LIM - 3) << 18);
    if (d < (LIM - 2) << 18) asm volatile("s_nop 7");
    if (d < (LIM - 1) << 18) asm volatile("s_nop 7");
    if (d < LIM << 18) asm volatile("s_nop 7");
}
DEVI void red_role(const Prm& p, rsrc_t r, const int x, const int a) {
    Ln l = mkln();
    page_touch(r, a);
    if (l.w > 1 || (l.w == 1 && a > 3)) return;
    const int unit = l.w == 0 ? (a < 8 ? 8 * x + a : 24 + 8 * x + a) : 64 + 4 * x + a, uj = unit >> 1;
    const bool lead = unit == 64;
    const rsrc_t rc = __builtin_amdgcn_make_buffer_rsrc(p.comm, 0, 4 * PAGE, 0x00020000);
    u32x4 ct;
    {
        const int jj = 2 * (2 * ((l.c16 >> 2) & 1) + (l.c16 & 1)) + ((l.c16 >> 1) & 1);
#pragma unroll
        for (int n = 0; n < 4; ++n) ct[n] = (jj >> 1) == n ? ((jj & 1) ? 0x3F800000u : 0x00003F80u) : 0u;
    }
    const unsigned po = (l.lane & 8) ? OOB : (l.g & 1) * 64 + (l.lane & 7) * 8 + (l.g >> 1) * 4;
    const Pg pg = mkpg(p, wait_ge(r, l.bud, 0, NINIT << 8));
    const unsigned go = (p.tA >> (8 * (unit >> 5))) & 255, mA = p.tA >> 24, D = p.tB & 255, Q = (p.dG >> 16) & 255, pre = (p.tB >> 26) * 2, thr = (p.tB >> 4) & (15 << 18);
    unsigned sl = (8192 ^ (pg.cs * 8192)) + unit * 16384, mb = (3 * PAGE) ^ (pg.cs * 8192);
    const unsigned rp = 32768 + (uj < 32 ? uj * 256 : 16384 + (uj - 32) * 256) + (unit & 1) * 128;
    unsigned A = (p.tB >> 8) & 511, An = A, T;
    const u32x4 sv = {SENTA, SENTA, SENTA, SENTA};
    auto reset = [&](unsigned o) {
#pragma unroll
        for (int n = 0; n < 8; ++n) st128(rc, l.lane * 16 + (n & 3) * 1024, o + (n >> 2) * 4096, sv);
    };
    reset(sl ^ (PAGE + 8192));
    if (unit >= 2 && unit < 64) {
#pragma unroll
        for (int n = 0; n < 12; ++n) st128(rc, l.lane * 16 + (n & 3) * 1024, mb + unit * 32768 + PH((n >> 2) * 4096), u32x4{SENT, SENT, SENT, SENT});
    }
    VM_WAIT0();
    wait_ge(r, l.bud, l.lane * 128 + pg.hf + F_M + 4, 1, 1);
#pragma unroll 1
    for (;;) {
        asm volatile("" ::: "memory");
        const unsigned f = ld32(rc, l.lane * 16, sl);
        if (__builtin_amdgcn_ballot_w64(f == SENTA) == 0 || l.bud == 0) break;
        --l.bud;
        __builtin_amdgcn_s_sleep(8);
    }
    T = rt18() - ((A - go) << 18);
    auto delay = [&]() {
        unsigned m = ld32(rc, l.p4 * 256, mb + 16384 + 2304);
        if (__builtin_amdgcn_ballot_w64(m != SENT) == 0) return 0u;
        m = min(m, (unsigned)__builtin_amdgcn_update_dpp(0u, m, 0x4e, 0xf, 0xf, true)); m = min(m, (unsigned)__builtin_amdgcn_update_dpp(0u, m, 0xb1, 0xf, 0xf, true));
        const unsigned d = ((~__builtin_amdgcn_readfirstlane(m) - A) << 18) - T;
        return d < LIM << 18 ? d : 0u;
    };
#pragma unroll 1
    for (int s = 0; s < 63; ++s) {
        unsigned lo = thr;
#pragma unroll 1
        for (unsigned ah = pre;; ah = T_RC) {
            until(T + ((A - go - ah) << 18));
            const unsigned d = delay();
            if (d < lo) break;
            T += d;
            if ((lo == 0 && d < T_RD << 18) || l.bud == 0) break;
            lo = 0;
            --l.bud;
        }
        until(T + ((A - go) << 18));
        unsigned np = 0;
#pragma unroll 1
        for (;; ++np) {
            u32x4 t[8]; unsigned o; unsigned long long ok;
            asm volatile(
                "buffer_load_dwordx4 %0, %10, %11, %12 offen offset:0 sc1\n" "buffer_load_dwordx4 %1, %10, %11, %12 offen offset:1024 sc1\n"
                "buffer_load_dwordx4 %2, %10, %11, %12 offen offset:2048 sc1\n" "buffer_load_dwordx4 %3, %10, %11, %12 offen offset:3072 sc1\n"
                "buffer_load_dwordx4 %4, %10, %11, %13 offen offset:0 sc1\n" "buffer_load_dwordx4 %5, %10, %11, %13 offen offset:1024 sc1\n"
                "buffer_load_dwordx4 %6, %10, %11, %13 offen offset:2048 sc1\n" "buffer_load_dwordx4 %7, %10, %11, %13 offen offset:3072 sc1\n"
                "s_waitcnt vmcnt(7)\n" "v_mfma_f32_16x16x32_bf16 v[60:63], %0, %14, 0\n"
                "s_waitcnt vmcnt(6)\n" "v_mfma_f32_16x16x32_bf16 v[60:63], %1, %14, v[60:63]\n"
                "s_waitcnt vmcnt(5)\n" "v_mfma_f32_16x16x32_bf16 v[60:63], %2, %14, v[60:63]\n"
                "s_waitcnt vmcnt(4)\n" "v_mfma_f32_16x16x32_bf16 v[60:63], %3, %14, v[60:63]\n"
                "s_waitcnt vmcnt(3)\n" "v_mfma_f32_16x16x32_bf16 v[60:63], %4, %14, v[60:63]\n"
                "s_waitcnt vmcnt(2)\n" "v_mfma_f32_16x16x32_bf16 v[60:63], %5, %14, v[60:63]\n"
                "s_waitcnt vmcnt(1)\n" "v_mfma_f32_16x16x32_bf16 v[60:63], %6, %14, v[60:63]\n"
                "s_waitcnt vmcnt(0)\n" "v_mfma_f32_16x16x32_bf16 v[60:63], %7, %14, v[60:63]\n"
                "s_nop 7\n" "v_permlane32_swap_b32 v60, v62\n"
                "v_permlane32_swap_b32 v61, v63\n" "v_add_f32 v60, v60, v62\n"
                "v_add_f32 v61, v61, v63\n" "v_cvt_pk_bf16_f32 %8, v60, v61\n"
                "v_cmpx_o_f32 vcc, v60, v61\n" "buffer_store_dword %8, %15, %11, %16 offen sc1\n"
                "s_mov_b64 exec, -1\n" "s_mov_b64 %9, vcc\n"
                : "=&v"(t[0]), "=&v"(t[1]), "=&v"(t[2]), "=&v"(t[3]), "=&v"(t[4]), "=&v"(t[5]), "=&v"(t[6]), "=&v"(t[7]), "=&v"(o), "=s"(ok)
                : "v"(l.lane * 16), "s"(rc), "s"(sl), "s"(sl + 4096), "v"(ct), "v"(po), "s"(mb + rp) : "memory", "vcc", "v60", "v61", "v62", "v63");
            if (ok == ~0ull) break;
            if ((np & 15) == 15 || l.bud == 0) {
                bool open = false;
#pragma unroll
                for (int n = 0; n < 32; ++n) open |= t[n >> 2][n & 3] == SENTA;
                if (__builtin_amdgcn_ballot_w64(open) == 0 || l.bud == 0) { st32(rc, po, mb + rp, min(o, 0xFFFFFFFEu)); break; }
            }
            --l.bud;
        }
        const unsigned now = lead ? rt18() : 0;
        if (lead && (np || lo == 0)) { const unsigned d = delay(); if (d >= lo) T += d; }
        T += (A + D) << 18;
        mb += 32768;
        if (lead) {
            if (np && (int)(now + (T_XL << 18) - T) > 0) T = now + ((T_XL + T_RX) << 18);
            else if (T - now + (T_TOL << 18) >= LIM << 18) T = now + (T_REC << 18);
            A = An;
            st32(rc, l.lane == 0 ? 0 : OOB, mb + 16384 + 2048, T | A);
        }
        until(T + (Q << 18));
        reset(sl);
        if (lead) {
            int m = ld32(r, l.lane * 128 + pg.hf + F_M + 4, 0);
            m = max(m, __builtin_amdgcn_update_dpp(0, m, 0x128, 0xf, 0xf, true)); m = max(m, __builtin_amdgcn_update_dpp(0, m, 0x124, 0xf, 0xf, true));
            m = max(m, __builtin_amdgcn_update_dpp(0, m, 0x122, 0xf, 0xf, true)); m = max(m, __builtin_amdgcn_update_dpp(0, m, 0x121, 0xf, 0xf, true));
            m = max(max(__builtin_amdgcn_readlane(m, 0), __builtin_amdgcn_readlane(m, 16)), max(__builtin_amdgcn_readlane(m, 32), __builtin_amdgcn_readlane(m, 48)));
            An = max(min(max(min(m + mA, An + T_UP), An - T_DN), 400u), 40u);
        } else {
#pragma unroll 1
            for (;;) {
                asm volatile("" ::: "memory");
                const unsigned f = __builtin_amdgcn_readfirstlane(ld32(rc, min(l.lane, 4) * 256, mb + 16384 + 2048));
                if (f != SENT) { T = f & 0xfffc0000u; A = f & 511; break; }
                if (l.bud == 0) break;
                --l.bud;
                __builtin_amdgcn_s_sleep(2);
            }
        }
        asm volatile(".Lk6:\n\ts_waitcnt vmcnt(0)" ::: "memory");
        sl ^= PAGE + 8192;
    }
}
__global__ void __launch_bounds__(256, 1) fused_kernel(Prm p) {
    extern __shared__ __attribute__((aligned(16))) uchar smem[];
    const int b = blockIdx.x, x = b & 7, q = b >> 3;
    const rsrc_t r = __builtin_amdgcn_make_buffer_rsrc(p.ws, 0, W_END + P_END, 0x00020000);
    Cd cd;
    cd.a = ld32(r, gw(128), G_ACK);
    {
        const uchar* pc; unsigned len;
        asm volatile("s_getpc_b64 %0\n.Lk0:\n\ts_mov_b32 %1, .Lk1-.Lk0\n\ts_max_u32 %1, %1, .Lk2-.Lk0\n\ts_max_u32 %1, %1, .Lk3-.Lk0\n\ts_max_u32 %1, %1, .Lk4-.Lk0\n\t"
                     "s_max_u32 %1, %1, .Lk5-.Lk0\n\ts_max_u32 %1, %1, .Lk6-.Lk0" : "=s"(pc), "=s"(len) :: "scc");
        cd.c = touch(pc + min((q * 32 + (threadIdx.x & 31)) * 128, len));
    }
    u32x4 az;
    if (b == B_Z) {
#pragma unroll
        for (int i = 0; i < F_KV / 4096; ++i) st128m(r, threadIdx.x * 16, i * 4096, u32x4{0, 0, 0, 0});
        az = ld128(r, threadIdx.x * 128, G_ACK);
        VM_BAR(1);
    }
    if (x < 4 && q < 16) { chain_role(p, r, 16 * x + q, cd); return; }
    if (q < 12) {
        if (q >= 4 && q < 8) kv_role<true>(p, r, 4 * (x - 4) + (q & 3), 1, cd); else kv_role<false>(p, r, 4 * (x - 4) + (q & 3), q >> 2, cd);
        if (q < 10) r2_role(p, r, 4 * q + x - 4);
    }
    if (q >= 12) {
        if (x < 4) init_role(p, r, x, q - 16, cd);
        else if (q >= 16) ln1_role(p, r, x - 4, q - 16, cd);
        else {
            if (b == B_Z) {
                echo(r, 18, az);
                asm volatile("" :: "v"(cd.c));
            } else {
                ask(r, cd);
                kv_touch(p, x - 4, q - 7, cd.c);
                gate(r, cd);
            }
            if (q < 15) mod_rest(p, r, x - 4, q - 12);
        }
    }
    if (x >= 4 && q >= 16 && 1) page_touch(r, q - 16);
    if (x < 4) red_role(p, r, x, q - 16);
    else if (q >= 24 || q == 15) ffn_role(p, r, q == 15 ? 14 + (x >> 1) : 4 * (x - 4) + ((q - 24) >> 1), (q == 15 ? x : q) & 1);
    else if (q >= 16) ao_role(p, r, 4 * (x - 4) + ((q - 16) >> 1), q & 1);
    else if (q >= 11) r1_role(p, r, 0, q == 11 ? 16 * S_TL + 8 + x : 4 * (q - 12) + x - 4);
}

static void launch(void* const* d_in, void* d_out, void* d_ws, hipStream_t stream, int dA, int dB, int dG, int flip) {
    uchar* ws = (uchar*)(((uintptr_t)d_ws + PAGE - 1) & ~(uintptr_t)(PAGE - 1));
    uchar* comm = ws + W_END;
    (void)hipFuncSetAttribute((const void*)fused_kernel, hipFuncAttributeMaxDynamicSharedMemorySize, LDS_BYTES);
    Prm p{(const uchar*)d_in[0], (const uchar*)d_in[1], (const uchar*)d_in[2], (const uchar*)d_in[3], (const uchar*)d_in[4], (const uchar*)d_in[5],
          (const uchar*)d_in[8], (const uchar*)d_in[9], (const uchar*)d_in[10], (const uchar*)d_in[11], (const uchar*)d_in[12],
          (uchar*)d_out, ws, comm, dA, dB, dG, flip};
    fused_kernel<<<256, 256, LDS_BYTES, stream>>>(p);
}

extern "C" void kernel_launch(void* const* d_in, const int* in_sizes, int n_in, void* d_out, int out_size, void* d_ws, size_t ws_size, hipStream_t stream) {
    launch(d_in, d_out, d_ws, stream, 117440773, 1569754154, 1310980, 0);
}
```
